```python
import torch

def _softmax(x, axis=-1):
    return torch.softmax(x, dim=axis)
def _gelu(x):
    return torch.nn.functional.gelu(x, approximate="tanh")
def _silu(x):
    return torch.nn.functional.silu(x)
def _sigmoid(x):
    return torch.sigmoid(x)
def _layernorm(x, axis=-1, eps=1e-5):
    m = x.mean(dim=axis, keepdim=True)
    v = x.var(dim=axis, keepdim=True, unbiased=False)
    return (x - m) / torch.sqrt(v + eps)
def _scatter_add(tgt, idx, src, axis=0):
    return tgt.clone().index_add_(axis, idx, src)
def _segment_sum(src, seg, n):
    out = torch.zeros((n,) + src.shape[1:], dtype=src.dtype)
    return out.index_add_(0, seg, src)
def _pad(x, pw):
    flat = tuple(v for pair in reversed(pw) for v in pair)
    return torch.nn.functional.pad(x, flat)

kernel_name = 'recurrent_scan__mha_block__geglu_ffn__prompt'

def setup_inputs():

    _w = (lambda a: (lambda t: t.to(torch.bfloat16) if t.is_floating_point() else t)(torch.as_tensor(a)))
    def _A_setup():
        B, S, D, H = 1, 2048, 448, 16
        return {
            "x": _w((lambda *s: torch.randn(*s, dtype=torch.bfloat16) * 0.05)(B, S, D)),
            "wqkv": _w((lambda *s: torch.randn(*s, dtype=torch.bfloat16) * 0.05)(D, 3 * D)),
            "wo": _w((lambda *s: torch.randn(*s, dtype=torch.bfloat16) * 0.05)(D, D)),
        }
    _A_in = _A_setup()

    def _B_setup():
        B, S, D, F = 1, 2048, 448, 1792
        return {
            "x": _w((lambda *s: torch.randn(*s, dtype=torch.bfloat16) * 0.05)(B, S, D)),
            "w_gate": _w((lambda *s: torch.randn(*s, dtype=torch.bfloat16) * 0.05)(D, F)),
            "w_up": _w((lambda *s: torch.randn(*s, dtype=torch.bfloat16) * 0.05)(D, F)),
            "w_down": _w((lambda *s: torch.randn(*s, dtype=torch.bfloat16) * 0.05)(F, D)),
        }
    _B_in = _B_setup()

    return {
        "A_x": _A_in["x"],
        "A_wqkv": _A_in["wqkv"],
        "A_wo": _A_in["wo"],
        "B_x": _B_in["x"],
        "B_w_gate": _B_in["w_gate"],
        "B_w_up": _B_in["w_up"],
        "B_w_down": _B_in["w_down"],
    }

def reference(A_x, A_wqkv, A_wo, B_x, B_w_gate, B_w_up, B_w_down):
    def _A_ref(x, wqkv, wo):
        B, S, D = x.shape
        H = 16
        Dh = D // H
        qkv = (lambda x, *d: x.permute(*d))((x @ wqkv).reshape(B, S, 3, H, Dh), 2, 0, 3, 1, 4)
        q, k, v = qkv[0], qkv[1], qkv[2]
        scores = torch.einsum("bhqd,bhkd->bhqk", q, k) * (Dh ** -0.5)
        p = _softmax(scores, axis=-1)
        o = torch.einsum("bhqk,bhkd->bhqd", p, v)
        return (lambda x, *d: x.permute(*d))(o, 0, 2, 1, 3).reshape(B, S, D) @ wo

    def _B_ref(x, w_gate, w_up, w_down):
        return (_gelu(x @ w_gate) * (x @ w_up)) @ w_down

    x = A_x
    xn = _layernorm(x, axis=-1)
    s = _A_ref(xn, A_wqkv, A_wo)
    g = _silu(_B_ref(xn, B_w_gate, B_w_up, B_w_down))
    return x + s * g
```

```cpp
#include <hip/hip_runtime.h>
#include <hip/hip_bf16.h>
typedef __hip_bfloat16 bf16;
constexpr int S=2048, D=448, H=16, DH=28, F=1792;
__device__ inline float b2f(bf16 x){return __bfloat162float(x);}
__device__ inline float rb(float x){return __bfloat162float(__float2bfloat16(x));}
__global__ void ln_k(const bf16* x, float* xn){
  int r=blockIdx.x; int t=threadIdx.x;
  float v[7]; float s=0; for(int i=0;i<7;i++){v[i]=b2f(x[r*D+t+64*i]); s+=v[i];}
  for(int o=32;o;o>>=1) s+=__shfl_xor(s,o);
  float m=s/D; float q=0; for(int i=0;i<7;i++){float d=v[i]-m;q+=d*d;}
  for(int o=32;o;o>>=1) q+=__shfl_xor(q,o);
  float rs=1.f/sqrtf(q/D+1e-5f);
  for(int i=0;i<7;i++) xn[r*D+t+64*i]=rb((v[i]-m)*rs);
}
__global__ void gemm_k(const float* A, const bf16* W, float* C, int M,int N,int K){
  __shared__ float As[16][17], Ws[16][17];
  int tx=threadIdx.x, ty=threadIdx.y; int r=blockIdx.y*16+ty, c=blockIdx.x*16+tx; float acc=0;
  for(int k0=0;k0<K;k0+=16){ As[ty][tx]=A[r*K+k0+tx]; Ws[ty][tx]=b2f(W[(k0+ty)*N+c]); __syncthreads();
    for(int k=0;k<16;k++) acc+=As[ty][k]*Ws[k][tx]; __syncthreads(); }
  C[r*N+c]=rb(acc);
}
__global__ void attn_k(const float* qkv, float* o){
  int q=blockIdx.x*64+threadIdx.x, h=blockIdx.y;
  float qv[DH], acc[DH]; for(int d=0;d<DH;d++){qv[d]=qkv[q*3*D+h*DH+d];acc[d]=0;}
  float m=-1e30f,l=0; const float sc=0.1889822365046136f;
  for(int k=0;k<S;k++){ float s=0; for(int d=0;d<DH;d++) s+=qv[d]*qkv[k*3*D+D+h*DH+d]; s*=sc;
    float mn=fmaxf(m,s); float a=__expf(m-mn), p=__expf(s-mn); l=l*a+p; for(int d=0;d<DH;d++) acc[d]=acc[d]*a+p*qkv[k*3*D+2*D+h*DH+d]; m=mn; }
  for(int d=0;d<DH;d++) o[q*D+h*DH+d]=rb(acc[d]/l);
}
__global__ void geglu_k(const float* g,const float* u,float* h,int n){ int i=blockIdx.x*256+threadIdx.x; if(i<n){ float x=g[i]; float t=tanhf(0.7978845608028654f*(x+0.044715f*x*x*x)); h[i]=rb(rb(0.5f*x*(1+t))*u[i]); } }
__global__ void fin_k(const bf16* x,const float* s,const float* f,bf16* out,int n){ int i=blockIdx.x*256+threadIdx.x; if(i<n){ float v=f[i]; float g=rb(v/(1+__expf(-v))); out[i]=__float2bfloat16(b2f(x[i])+rb(s[i]*g)); } }
extern "C" void kernel_launch(void* const* d_in, const int* in_sizes, int n_in, void* d_out, int out_size, void* d_ws, size_t ws_size, hipStream_t stream){
  const bf16* x=(const bf16*)d_in[0]; const bf16* wqkv=(const bf16*)d_in[1]; const bf16* wo=(const bf16*)d_in[2];
  const bf16* wg=(const bf16*)d_in[4]; const bf16* wu=(const bf16*)d_in[5]; const bf16* wd=(const bf16*)d_in[6];
  float* ws=(float*)d_ws; float* xn=ws; float* qkv=xn+S*D; float* o=qkv+S*3*D; float* s=o+S*D; float* g=s+S*D; float* u=g+S*F; float* h=u+S*F; float* f=h+S*F;
  ln_k<<<S,64,0,stream>>>(x,xn);
  gemm_k<<<dim3(3*D/16,S/16),dim3(16,16),0,stream>>>(xn,wqkv,qkv,S,3*D,D);
  attn_k<<<dim3(S/64,H),64,0,stream>>>(qkv,o);
  gemm_k<<<dim3(D/16,S/16),dim3(16,16),0,stream>>>(o,wo,s,S,D,D);
  gemm_k<<<dim3(F/16,S/16),dim3(16,16),0,stream>>>(xn,wg,g,S,F,D);
  gemm_k<<<dim3(F/16,S/16),dim3(16,16),0,stream>>>(xn,wu,u,S,F,D);
  geglu_k<<<(S*F+255)/256,256,0,stream>>>(g,u,h,S*F);
  gemm_k<<<dim3(D/16,S/16),dim3(16,16),0,stream>>>(h,wd,f,S,D,F);
  fin_k<<<(S*D+255)/256,256,0,stream>>>(x,s,f,(bf16*)d_out,S*D);
}
```

```cpp
#include <hip/hip_runtime.h>
#include <stdint.h>
#include <type_traits>

typedef short bf16x8 __attribute__((ext_vector_type(8)));
typedef short s16x4 __attribute__((ext_vector_type(4)));
typedef float f32x16 __attribute__((ext_vector_type(16)));
typedef float f32x4 __attribute__((ext_vector_type(4)));
typedef float f32x2 __attribute__((ext_vector_type(2)));
typedef unsigned u32x4 __attribute__((ext_vector_type(4)));
typedef unsigned u32x2 __attribute__((ext_vector_type(2)));
typedef __bf16 bf16x2_t __attribute__((ext_vector_type(2)));
typedef __amdgpu_buffer_rsrc_t rsrc_t;
typedef unsigned long long u64;
#define LAS __attribute__((address_space(3)))
#define GAS __attribute__((address_space(1)))
#define DI __device__ __forceinline__
#define MFMA32(a, b, c) __builtin_amdgcn_mfma_f32_32x32x16_bf16((a), (b), (c), 0, 0, 0)
#define MFMA16(a, b, c) __builtin_amdgcn_mfma_f32_16x16x32_bf16((a), (b), (c), 0, 0, 0)

constexpr int SEQ = 2048, DM = 448, FF = 1792;
constexpr unsigned KC_OFF = 0, QC_OFF = 0x200000, VT_OFF = 0x400000, HF_OFF = 0x600000, OF_OFF = 0xD00000, FL_OFF = 0xF00000,
                   EP_OFF = FL_OFF + 8192, ST_OFF = FL_OFF + 0x10000, WS_BYTES = 0x1000000;
constexpr int LDS_BYTES = 163840;
constexpr int AUX_SC1 = 16;
constexpr unsigned SLOT1 = 40960;
constexpr unsigned TAB = 155648;
constexpr unsigned T_SP = TAB;
constexpr unsigned T_CP = TAB + 4096;
constexpr unsigned T_S = TAB + 7168;
constexpr unsigned T_C = 32768;
constexpr unsigned VSCR = 16384;
__host__ __device__ constexpr unsigned p2_slot(int c) { return c == 0 ? 122880u : c == 1 ? 139264u : c == 2 ? 40960u : c == 3 ? 57344u : c == 4 ? 81920u : c == 5 ? 98304u : 0u; }

template <int I, int N, class F> DI void sfor(F&& f) { if constexpr (I < N) { f(std::integral_constant<int, I>{}); sfor<I + 1, N>(f); } }
DI rsrc_t mk_rsrc(const void* p, unsigned bytes) { return __builtin_amdgcn_make_buffer_rsrc((void*)p, (short)0, (int)bytes, 0x00020000); }
DI void dma(unsigned voff, rsrc_t r, unsigned soff, unsigned lds) {
    asm volatile("s_mov_b32 m0, %3\n\ts_nop 0\n\tbuffer_load_dwordx4 %0, %1, %2 offen lds" ::"v"(voff), "s"(r), "s"(soff), "s"(lds) : "memory");
}
DI void dma_sc1(unsigned voff, rsrc_t r, unsigned soff, unsigned lds) {
    asm volatile("s_mov_b32 m0, %3\n\ts_nop 0\n\tbuffer_load_dwordx4 %0, %1, %2 offen sc1 lds" ::"v"(voff), "s"(r), "s"(soff), "s"(lds) : "memory");
}
DI void dma_sc1_m28(unsigned voff, rsrc_t r, unsigned soff, unsigned lds) {
    asm volatile("s_mov_b32 m0, %3\n\ts_mov_b32 exec_lo, 0x0fffffff\n\ts_mov_b32 exec_hi, 0x0fffffff\n\tbuffer_load_dwordx4 %0, %1, %2 offen sc1 lds\n\ts_mov_b64 exec, -1"
                 ::"v"(voff), "s"(r), "s"(soff), "s"(lds) : "memory");
}
template <int N> DI void wait_bar() { asm volatile("s_waitcnt vmcnt(%0) lgkmcnt(0)\n\ts_barrier" ::"n"(N) : "memory"); }
DI void bar_lds() { asm volatile("s_waitcnt lgkmcnt(0)\n\ts_barrier" ::: "memory"); }
DI bf16x8 lds_frag(unsigned a) { return *(const LAS bf16x8*)(uintptr_t)a; }
DI s16x4 lds_tr(unsigned a) { return __builtin_amdgcn_ds_read_tr16_b64_v4i16((LAS s16x4*)(uintptr_t)a); }
DI bf16x8 lds_tr2(unsigned a0, unsigned a1) { s16x4 lo = lds_tr(a0), hi = lds_tr(a1); return __builtin_shufflevector(lo, hi, 0, 1, 2, 3, 4, 5, 6, 7); }
DI f32x4 lds_f4(unsigned a) { return *(const LAS f32x4*)(uintptr_t)a; }
DI unsigned pk(float lo, float hi) { f32x2 v = {lo, hi}; return __builtin_bit_cast(unsigned, __builtin_convertvector(v, bf16x2_t)); }
DI float bfr(float x) { return __uint_as_float(pk(x, x) & 0xffff0000u); }
DI float blo(unsigned d) { return __uint_as_float(d << 16); }
DI float bhi(unsigned d) { return __uint_as_float(d & 0xffff0000u); }
DI void st16(u32x4 v, rsrc_t r, unsigned voff, unsigned soff) { __builtin_amdgcn_raw_buffer_store_b128(v, r, voff, soff, AUX_SC1); }
DI u32x4 ld16(rsrc_t r, unsigned voff, unsigned soff) { return __builtin_amdgcn_raw_buffer_load_b128(r, voff, soff, AUX_SC1); }
DI void swap32(unsigned& a, unsigned& b) { auto t = __builtin_amdgcn_permlane32_swap(a, b, false, false); a = t[0]; b = t[1]; }

DI void set_flag(char* ws, unsigned off, u64 tag) { __hip_atomic_store((GAS u64*)(ws + off), tag, __ATOMIC_RELAXED, __HIP_MEMORY_SCOPE_AGENT); }
DI void wait_flags(char* ws, unsigned off, u64 tag) {
    for (unsigned spins = 0; spins < (1u << 18); ++spins) {
        const u64 v = __hip_atomic_load((GAS u64*)(ws + off), __ATOMIC_RELAXED, __HIP_MEMORY_SCOPE_AGENT);
        if (__all(v == tag)) break;
        __builtin_amdgcn_s_sleep(2);
    }
    asm volatile("" ::: "memory");
}
__host__ __device__ constexpr unsigned fl(int kind, int a, int b) { return FL_OFF + kind * 2048 + (a * 16 + b) * 8; }

#define STAMP(i) do {} while (0)

DI void epi_kq(const f32x16& acc, unsigned ctab, float mean, float sc, int hh, rsrc_t rW, unsigned voff, unsigned soff) {
    unsigned p[8];
#pragma unroll
    for (int g = 0; g < 4; ++g) { const f32x4 c = lds_f4(ctab + g * 32 + hh * 16);
        p[2 * g] = pk((acc[4 * g] - mean * c[0]) * sc, (acc[4 * g + 1] - mean * c[1]) * sc);
        p[2 * g + 1] = pk((acc[4 * g + 2] - mean * c[2]) * sc, (acc[4 * g + 3] - mean * c[3]) * sc); }
#pragma unroll
    for (int g = 0; g < 4; g += 2) { swap32(p[2 * g], p[2 * g + 2]); swap32(p[2 * g + 1], p[2 * g + 3]);
        st16((u32x4){p[2 * g], p[2 * g + 1], p[2 * g + 2], p[2 * g + 3]}, rW, voff, soff + g * 32768); }
}
DI void epi_h(const f32x16& aG, const f32x16& aU, unsigned cG, unsigned cU, float mean, float rstd, int hh, rsrc_t rW, unsigned voff, unsigned soff) {
    const float k1 = -2.302208198144325f * rstd;
    const float k2 = k1 * 0.044715f * rstd * rstd;
    const float k3 = 1.0f / (rstd * rstd);
    float h[16];
#pragma unroll
    for (int g = 0; g < 4; ++g) { const f32x4 c = lds_f4(cG + g * 32 + hh * 16), d = lds_f4(cU + g * 32 + hh * 16);
#pragma unroll
        for (int e = 0; e < 4; ++e) { const float G = __builtin_fmaf(-mean, c[e], aG[4 * g + e]), U = __builtin_fmaf(-mean, d[e], aU[4 * g + e]);
            const float z = G * __builtin_fmaf(G * G, k2, k1);
            const float r = __builtin_amdgcn_rcpf(__builtin_fmaf(__builtin_amdgcn_exp2f(z), k3, k3));
            h[4 * g + e] = (G * U) * r; } }
#pragma unroll
    for (int s = 0; s < 2; ++s)
        st16((u32x4){pk(h[8 * s], h[8 * s + 1]), pk(h[8 * s + 2], h[8 * s + 3]), pk(h[8 * s + 4], h[8 * s + 5]), pk(h[8 * s + 6], h[8 * s + 7])}, rW, voff, soff + s * 1024);
}

template <int PH, bool SYNC>
__global__ __launch_bounds__(512, 2) void fused_kernel(const void* __restrict__ x, const void* __restrict__ wqkv, const void* __restrict__ wo, const void* __restrict__ wg,
                                                       const void* __restrict__ wu, const void* __restrict__ wd, void* __restrict__ out, char* ws) {
    extern __shared__ __attribute__((aligned(16))) char smem[];
    const unsigned L = (unsigned)(uintptr_t)(LAS char*)smem;
    const int tid = threadIdx.x, lane = tid & 63, w = __builtin_amdgcn_readfirstlane(tid >> 6);
    const int r32 = lane & 31, hh = lane >> 5;
    const int xcd = blockIdx.x & 7, li = blockIdx.x >> 3;
    const int j = 4 * (xcd & 3) + (li & 3), rb = 8 * (xcd >> 2) + (li >> 2);
    const rsrc_t rW = mk_rsrc(ws, WS_BYTES), rX = mk_rsrc(x, SEQ * DM * 2);
    u64 tag = 0;
    if (SYNC) { const u64 e = __hip_atomic_load((GAS u64*)(ws + EP_OFF), __ATOMIC_RELAXED, __HIP_MEMORY_SCOPE_AGENT); tag = e + 1; }
    const unsigned troff = ((lane >> 2) & 3) * 64 + ((lane >> 4) & 1) * 32 + (lane & 3) * 8;
    const unsigned drow = 4 * hh + ((lane >> 2) & 3), dcol = ((lane >> 4) & 1) * 64 + (lane & 3) * 16;
    STAMP(0);

    if constexpr (PH & 1) {
        const int tb = w & 3, ch = w >> 2, rot = tb ^ (ch << 1);
        const bool qrole = j >= 14;
        const rsrc_t r0 = mk_rsrc(qrole ? wqkv : wg, qrole ? DM * 3 * DM * 2 : DM * FF * 2), r1 = mk_rsrc(qrole ? wqkv : wu, qrole ? DM * 3 * DM * 2 : DM * FF * 2),
                     r2 = mk_rsrc(wqkv, DM * 3 * DM * 2);
        const unsigned strA = qrole ? 2688u : 3584u;
        const unsigned c0a = qrole ? (j == 14 ? 0u : 512u) : 256u * j, c1a = qrole ? c0a + 128u : c0a, c2a = qrole ? c0a + 256u : 896u + 128u * j;
        const unsigned cb = qrole ? 384u : 256u * j + 128u;
        const unsigned woffA = (8 * w + drow) * strA + dcol, woffB = (8 * w + drow) * 2688u + dcol;
        const unsigned xr = 16 * w + (lane >> 3);
        const unsigned xoffa = xr * 896u + (((lane & 7) ^ ((xr >> 1) & 7)) << 4), xoffb = (xr + 8) * 896u + (((lane & 7) ^ (((xr + 8) >> 1) & 7)) << 4);
        const unsigned xs0 = rb * 128 * 896;
        auto issue1 = [&](int c) { const unsigned sl = L + (c % 3) * SLOT1;
            dma(xoffa, rX, xs0 + c * 128, sl + w * 2048); dma(xoffb, rX, xs0 + c * 128, sl + w * 2048 + 1024);
            dma(woffA, r0, c0a + c * 64 * strA, sl + 16384 + w * 3072); dma(woffA, r1, c1a + c * 64 * strA, sl + 16384 + w * 3072 + 1024);
            dma(woffB, r2, c2a + c * 64 * 2688, sl + 16384 + w * 3072 + 2048); };
        auto issue2 = [&](int c) { const unsigned sl = L + p2_slot(c);
            dma(woffA, r0, cb + c * 64 * strA, sl + w * 2048); dma(woffA, r1, cb + c * 64 * strA, sl + w * 2048 + 1024); };
        issue1(0); issue1(1); issue1(2); issue2(0); issue2(1);

        unsigned xa[4], wa[4];
#pragma unroll
        for (int i = 0; i < 4; ++i) { const int s = i ^ rot;
            xa[i] = L + (tb * 32 + r32) * 128 + (((2 * s + hh) ^ ((r32 >> 1) & 7)) << 4);
            wa[i] = L + 16384 + (2 * s + hh) * 3072 + ch * 256 + troff; }
        const unsigned selw = ((lane & 15) == ((lane >> 4) & 1)) ? 0x3F803F80u : 0u;
        const bf16x8 sel = __builtin_bit_cast(bf16x8, (u32x4){selw, selw, selw, selw});
        bf16x8 xf[28];
        f32x16 a0 = {}, a1 = {}, a2 = {};
        f32x4 cs0 = {}, cs1 = {}, cs2 = {};
        float s1a = 0.f, s1b = 0.f, s2a = 0.f, s2b = 0.f;
        sfor<0, 7>([&](auto C) { constexpr int c = C;
            wait_bar<(c == 0 ? 14 : c <= 2 ? 9 : c <= 5 ? 5 : 4)>();
            if constexpr (c >= 1 && c <= 4) issue1(c + 2);
            if constexpr (c == 5) { issue2(2); issue2(3); }
            if constexpr (c == 6) { issue2(4); issue2(5); }
            constexpr unsigned sl = (c % 3) * SLOT1;
#pragma unroll
            for (int i = 0; i < 4; ++i) {
                const bf16x8 xv = lds_frag(xa[i] + sl);
                const bf16x8 w0 = lds_tr2(wa[i] + sl, wa[i] + sl + 512), w1 = lds_tr2(wa[i] + sl + 1024, wa[i] + sl + 1536), w2 = lds_tr2(wa[i] + sl + 2048, wa[i] + sl + 2560);
                xf[4 * c + i] = xv;
                a0 = MFMA32(w0, xv, a0); a1 = MFMA32(w1, xv, a1); a2 = MFMA32(w2, xv, a2);
                if (i == 0) { cs0 = MFMA16(w0, sel, cs0); cs1 = MFMA16(w1, sel, cs1); cs2 = MFMA16(w2, sel, cs2); }
                if (i < 2) { const u32x4 d = __builtin_bit_cast(u32x4, xv);
#pragma unroll
                    for (int e = 0; e < 4; ++e) { const float lo = blo(d[e]), hi = bhi(d[e]); s1a += lo; s1b += hi; s2a = __builtin_fmaf(lo, lo, s2a); s2b = __builtin_fmaf(hi, hi, s2b); } }
            }
        });
        STAMP(1);
        *(LAS f32x2*)(uintptr_t)(L + T_SP + ((ch * 2 + hh) * 128 + tb * 32 + r32) * 8) = (f32x2){s1a + s1b, s2a + s2b};
        const unsigned cpo = L + T_CP + tb * 768 + ch * 128 + (lane & 15) * 64 + (lane >> 4) * 16;
        if ((lane & 15) < 2) { *(LAS f32x4*)(uintptr_t)(cpo) = cs0; *(LAS f32x4*)(uintptr_t)(cpo + 256) = cs1; *(LAS f32x4*)(uintptr_t)(cpo + 512) = cs2; }
        bar_lds();
        issue2(6);
        if (tid < 128) {
            const LAS f32x2* p = (const LAS f32x2*)(uintptr_t)(L + T_SP + tid * 8);
            const f32x2 t = (p[0] + p[128]) + (p[256] + p[384]);
            const float mean = t[0] * (1.0f / DM), var = __builtin_fmaxf(t[1] * (1.0f / DM) - mean * mean, 0.f);
            const float sd = bfr(__builtin_sqrtf(bfr(bfr(var) + 1e-5f)));
            *(LAS f32x2*)(uintptr_t)(L + T_S + tid * 8) = (f32x2){bfr(mean), 1.0f / sd};
        } else if (tid < 320) {
            const LAS float* p = (const LAS float*)(uintptr_t)(L + T_CP + (tid - 128) * 4);
            *(LAS float*)(uintptr_t)(L + T_C + (tid - 128) * 4) = (p[0] + p[192]) + (p[384] + p[576]);
        }
        bar_lds();
        const f32x2 st = *(const LAS f32x2*)(uintptr_t)(L + T_S + (tb * 32 + r32) * 8);
        const float mean = st[0], rstd = st[1];
        const unsigned ct = L + T_C + ch * 128;
        const unsigned tok = rb * 128 + tb * 32;
        constexpr float QS = 0.1889822365046136f * 1.4426950408889634f;
        if (qrole) {
            const unsigned vo = (hh * 2048 + r32) * 16, so = QC_OFF + (((c0a >> 4) + 4 * ch) * 2048 + tok) * 16;
            epi_kq(a0, ct, mean, rstd * QS, hh, rW, vo, so); epi_kq(a1, ct + 256, mean, rstd * QS, hh, rW, vo, so + 8 * 32768); epi_kq(a2, ct + 512, mean, rstd * QS, hh, rW, vo, so + 16 * 32768);
        } else {
            if (j < 7) epi_kq(a2, ct + 512, mean, rstd, hh, rW, (hh * 2048 + r32) * 16, KC_OFF + ((8 * j + 4 * ch) * 2048 + tok) * 16);
            else {
                const unsigned sc = L + VSCR + w * 2048;
#pragma unroll
                for (int g = 0; g < 4; ++g) { const f32x4 c = lds_f4(ct + 512 + g * 32 + hh * 16);
                    *(LAS u32x2*)(uintptr_t)(sc + r32 * 64 + g * 16 + hh * 8) = (u32x2){pk((a2[4 * g] - mean * c[0]) * rstd, (a2[4 * g + 1] - mean * c[1]) * rstd),
                                                                                       pk((a2[4 * g + 2] - mean * c[2]) * rstd, (a2[4 * g + 3] - mean * c[3]) * rstd)}; }
                asm volatile("s_waitcnt lgkmcnt(0)" ::: "memory");
#pragma unroll
                for (int s = 0; s < 2; ++s) { const unsigned a = sc + s * 1024 + hh * 256 + troff;
                    const bf16x8 v = lds_tr2(a, a + 512);
                    st16(__builtin_bit_cast(u32x4, v), rW, r32 * 32 + hh * 16, VT_OFF + (((tok >> 4) + s) * 448 + 64 * (j - 7) + 32 * ch) * 32); }
            }
            epi_h(a0, a1, ct, ct + 256, mean, rstd, hh, rW, lane * 16, HF_OFF + ((tok >> 5) * 112 + 8 * j + 2 * ch) * 1024);
        }
        STAMP(2);
#pragma unroll
        for (int i = 0; i < 4; ++i) wa[i] = L + (2 * (i ^ rot) + hh) * 2048 + ch * 256 + troff;
        a0 = f32x16{}; a1 = f32x16{}; cs0 = f32x4{}; cs1 = f32x4{};
        sfor<0, 7>([&](auto C) { constexpr int c = C;
            if constexpr (c == 2) { wait_bar<0>(); if (SYNC && tid == 0) set_flag(ws, fl(0, 0, 0) + (j * 16 + rb) * 8, tag); }
            constexpr unsigned sl = p2_slot(c);
#pragma unroll
            for (int i = 0; i < 4; ++i) {
                const bf16x8 w0 = lds_tr2(wa[i] + sl, wa[i] + sl + 512), w1 = lds_tr2(wa[i] + sl + 1024, wa[i] + sl + 1536);
                a0 = MFMA32(w0, xf[4 * c + i], a0); a1 = MFMA32(w1, xf[4 * c + i], a1);
                if (i == 0) { cs0 = MFMA16(w0, sel, cs0); cs1 = MFMA16(w1, sel, cs1); }
            }
        });
        STAMP(3);
        if ((lane & 15) < 2) { *(LAS f32x4*)(uintptr_t)(cpo) = cs0; *(LAS f32x4*)(uintptr_t)(cpo + 256) = cs1; }
        bar_lds();
        if (tid < 128) { const LAS float* p = (const LAS float*)(uintptr_t)(L + T_CP + tid * 4);
            *(LAS float*)(uintptr_t)(L + T_C + tid * 4) = (p[0] + p[192]) + (p[384] + p[576]); }
        bar_lds();
        if (qrole) { if (j == 14) epi_kq(a0, ct, mean, rstd * QS, hh, rW, (hh * 2048 + r32) * 16, QC_OFF + ((24 + 4 * ch) * 2048 + tok) * 16); }
        else epi_h(a0, a1, ct, ct + 256, mean, rstd, hh, rW, lane * 16, HF_OFF + ((tok >> 5) * 112 + 8 * j + 4 + 2 * ch) * 1024);
        wait_bar<0>();
        if (SYNC && tid == 0) set_flag(ws, fl(1, 0, 0) + (rb * 16 + j) * 8, tag);
        STAMP(4);
    }

    if constexpr (PH & 2) {
        const int qb = w & 3, kh = w >> 2, c0 = (7 * j) >> 1;
        constexpr unsigned AR = 0;
        if (SYNC && w == 0) {
            const int wh = lane >> 4;
            const int sl = wh == 0 ? (c0 >> 3) : wh == 1 ? ((c0 + 3) >> 3) : wh == 2 ? 7 + ((28 * j) >> 6) : 7 + ((28 * j + 27) >> 6);
            wait_flags(ws, fl(0, 0, 0) + (sl * 16 + (lane & 15)) * 8, tag);
            wait_flags(ws, lane == 0 ? fl(0, 14, 0) + rb * 8 : lane == 1 ? fl(0, 15, 0) + rb * 8 : fl(1, 0, 14) + rb * 128, tag);
        }
        if (r32 >= 28) { const unsigned v = r32 == 28 ? 0x3F803F80u : 0u;
#pragma unroll
            for (int sl = 0; sl < 3; ++sl) *(LAS u32x4*)(uintptr_t)(L + AR + sl * 16384 + (w >> 1) * 4096 + 2048 + (w & 1) * 1024 + lane * 16) = (u32x4){v, v, v, v}; }
        bar_lds();
        STAMP(5);
        const unsigned voK = (hh * 2048 + r32) * 16, voV = r32 * 32 + hh * 16;
        const unsigned key0 = (w >> 2) * 1024 + ((w >> 1) & 1) * 32;
        const unsigned soK = KC_OFF + ((c0 + 2 * (w & 1)) * 2048 + key0) * 16, soV = VT_OFF + (((key0 >> 4) + (w & 1)) * 448 + 28 * j) * 32;
        const unsigned ldK = __builtin_amdgcn_readfirstlane(L + AR + (w >> 1) * 4096 + (w & 1) * 1024);
        auto issueA = [&](int st, unsigned slot) { dma_sc1(voK, rW, soK + st * 1024, ldK + slot); dma_sc1_m28(voV, rW, soV + st * (4 * 448 * 32), ldK + slot + 2048); };
        const unsigned qso = QC_OFF + (c0 * 2048 + rb * 128 + qb * 32) * 16;
        u32x4 q0 = ld16(rW, voK, qso), q1 = ld16(rW, voK, qso + 2 * 32768);
        issueA(0, 0); issueA(1, 16384);
        if (j & 1) { if (hh == 0) { q0[0] = 0; q0[1] = 0; } } else { if (hh == 1) { q1[2] = 0; q1[3] = 0; } }
        const bf16x8 qf0 = __builtin_bit_cast(bf16x8, q0), qf1 = __builtin_bit_cast(bf16x8, q1);
        f32x16 o = {};
        unsigned sc = 0, sn = 32768;
        const unsigned rbase = L + AR + kh * 8192 + lane * 16;
#pragma unroll 1
        for (int st = 0; st < 16; ++st) {
            if (st < 15) wait_bar<2>(); else wait_bar<0>();
            if (st < 14) issueA(st + 2, sn);
            const unsigned a = rbase + sc;
#pragma unroll
            for (int t = 0; t < 2; ++t) {
                const bf16x8 k0 = lds_frag(a + t * 4096), k1 = lds_frag(a + t * 4096 + 1024), v0 = lds_frag(a + t * 4096 + 2048), v1 = lds_frag(a + t * 4096 + 3072);
                f32x16 s = MFMA32(k0, qf0, f32x16{}); s = MFMA32(k1, qf1, s);
                unsigned p[8];
#pragma unroll
                for (int e = 0; e < 8; ++e) p[e] = pk(__builtin_amdgcn_exp2f(s[2 * e]), __builtin_amdgcn_exp2f(s[2 * e + 1]));
                o = MFMA32(v0, __builtin_bit_cast(bf16x8, (u32x4){p[0], p[1], p[2], p[3]}), o);
                o = MFMA32(v1, __builtin_bit_cast(bf16x8, (u32x4){p[4], p[5], p[6], p[7]}), o);
            }
            sn = sc; sc = sc == 32768 ? 0 : sc + 16384;
        }
        STAMP(6);
        constexpr unsigned XO = 49152;
        unsigned lb = __float_as_uint(o[12]), lb2 = lb; swap32(lb, lb2);
        float l = __uint_as_float(lb2);
        float mine[8];
        { f32x4 g0, g1;
#pragma unroll
          for (int e = 0; e < 4; ++e) { g0[e] = kh ? o[e] : o[8 + e]; g1[e] = kh ? o[4 + e] : o[12 + e]; mine[e] = kh ? o[8 + e] : o[e]; mine[4 + e] = kh ? o[12 + e] : o[4 + e]; }
          const unsigned xw = L + XO + w * 4096 + lane * 16;
          *(LAS f32x4*)(uintptr_t)xw = g0; *(LAS f32x4*)(uintptr_t)(xw + 1024) = g1; *(LAS float*)(uintptr_t)(L + XO + w * 4096 + 2048 + lane * 4) = l; }
        bar_lds();
        { const unsigned xr = L + XO + (w ^ 4) * 4096 + lane * 16; const f32x4 g0 = lds_f4(xr), g1 = lds_f4(xr + 1024);
          l += *(const LAS float*)(uintptr_t)(L + XO + (w ^ 4) * 4096 + 2048 + lane * 4);
#pragma unroll
          for (int e = 0; e < 4; ++e) { mine[e] += g0[e]; mine[4 + e] += g1[e]; } }
        if (__builtin_expect(!__all(l >= 0x1p-60f && l <= 0x1p60f), 0)) {
            f32x16 oo = {}; float m = -INFINITY;
            const unsigned one = r32 == 28 ? 0x3F803F80u : 0u;
#pragma unroll 1
            for (int kt = 0; kt < 64; ++kt) {
                const unsigned sk = KC_OFF + (c0 * 2048 + kt * 32) * 16, sv = VT_OFF + (kt * 2 * 448 + 28 * j) * 32;
                const u32x4 k0 = ld16(rW, voK, sk), k1 = ld16(rW, voK, sk + 2 * 32768);
                u32x4 v0 = ld16(rW, voV, sv), v1 = ld16(rW, voV, sv + 448 * 32);
                if (r32 >= 28) { v0 = (u32x4){one, one, one, one}; v1 = v0; }
                f32x16 s = MFMA32(__builtin_bit_cast(bf16x8, k0), qf0, f32x16{}); s = MFMA32(__builtin_bit_cast(bf16x8, k1), qf1, s);
                float tm = s[0];
#pragma unroll
                for (int e = 1; e < 16; ++e) tm = __builtin_fmaxf(tm, s[e]);
                unsigned ta = __float_as_uint(tm), tb2 = ta; swap32(ta, tb2);
                const float mn = __builtin_fmaxf(m, __builtin_fmaxf(__uint_as_float(ta), __uint_as_float(tb2)));
                const float al = __builtin_amdgcn_exp2f(m - mn); m = mn;
                unsigned p[8];
#pragma unroll
                for (int e = 0; e < 8; ++e) p[e] = pk(__builtin_amdgcn_exp2f(s[2 * e] - mn), __builtin_amdgcn_exp2f(s[2 * e + 1] - mn));
                oo *= al;
                oo = MFMA32(__builtin_bit_cast(bf16x8, v0), __builtin_bit_cast(bf16x8, (u32x4){p[0], p[1], p[2], p[3]}), oo);
                oo = MFMA32(__builtin_bit_cast(bf16x8, v1), __builtin_bit_cast(bf16x8, (u32x4){p[4], p[5], p[6], p[7]}), oo);
            }
            unsigned la = __float_as_uint(oo[12]), lc = la; swap32(la, lc); l = __uint_as_float(lc);
#pragma unroll
            for (int e = 0; e < 8; ++e) mine[e] = kh ? oo[8 + e] : oo[e];
        }
        const float rl = 1.0f / l;
        u32x4 ov = {pk(mine[0] * rl, mine[1] * rl), pk(mine[2] * rl, mine[3] * rl), pk(mine[4] * rl, mine[5] * rl), pk(mine[6] * rl, mine[7] * rl)};
        if (kh && hh) { ov[2] = 0; ov[3] = 0; }
        st16(ov, rW, lane * 16, OF_OFF + ((rb * 4 + qb) * 32 + 2 * j + kh) * 1024);
        wait_bar<0>();
        if (SYNC && tid == 0) set_flag(ws, fl(2, 0, 0) + (rb * 16 + j) * 8, tag);
        STAMP(7);
    }

    if constexpr (PH & 4) {
        if (li < 28) {
            const int t64 = 4 * xcd + li / 7, cs = li % 7, rbD = t64 >> 1;
            const int tb = w & 1, kq = w >> 1, T = t64 * 2 + tb;
            const rsrc_t rD = mk_rsrc(wd, FF * DM * 2), rO = mk_rsrc(wo, DM * DM * 2), rOut = mk_rsrc(out, SEQ * DM * 2);
            constexpr unsigned WOB = 0, DR = 65536, RED = 65536;
            const unsigned voff = drow * 896 + dcol;
            auto issueD = [&](int e) { const unsigned so = cs * 128 + (448 * kq + 32 * e + 16 * tb) * 896, ld = L + DR + (e & 3) * 16384 + kq * 4096 + tb * 2048;
                dma(voff, rD, so, ld); dma(voff, rD, so + 8 * 896, ld + 1024); };
            issueD(0); issueD(1); issueD(2);
#pragma unroll
            for (int b = 0; b < 7; ++b) dma(voff, rO, cs * 128 + (w * 7 + b) * 8 * 896, L + WOB + (w * 7 + b) * 1024);
            if (tid < 32) *(LAS u32x4*)(uintptr_t)(L + WOB + 57344 + tid * 16) = (u32x4){0, 0, 0, 0};
            if (SYNC) { if (w == 0) wait_flags(ws, fl(1, 0, 0) + (rbD * 16 + (lane < 14 ? lane : 0)) * 8, tag); bar_lds(); }
            STAMP(8);
            u32x4 hf[28];
            const unsigned hso = HF_OFF + (T * 112 + 28 * kq) * 1024;
#pragma unroll
            for (int i = 0; i < 28; ++i) hf[i] = ld16(rW, lane * 16, hso + i * 1024);
            const unsigned col = cs * 64 + (kq >> 1) * 32 + (kq & 1) * 16 + 4 * hh, xo = ((T * 32 + r32) * 448 + col) * 2;
            const u32x2 xv0 = __builtin_amdgcn_raw_buffer_load_b64(rX, xo, 0, 0), xv1 = __builtin_amdgcn_raw_buffer_load_b64(rX, xo + 16, 0, 0);
            asm volatile("s_waitcnt vmcnt(0)" ::: "memory");
            f32x16 f0 = {}, f1 = {};
            const unsigned da = L + DR + kq * 4096 + hh * 512 + troff;
            sfor<0, 14>([&](auto E) { constexpr int e = E;
                wait_bar<(e < 12 ? 4 : e == 12 ? 2 : 0)>();
                if constexpr (e + 3 < 14) issueD(e + 3);
#pragma unroll
                for (int i = 0; i < 2; ++i) { const unsigned a = da + (e & 3) * 16384 + i * 2048;
                    const bf16x8 w0 = lds_tr2(a, a + 1024), w1 = lds_tr2(a + 256, a + 1280); const bf16x8 hv = __builtin_bit_cast(bf16x8, hf[2 * e + i]);
                    f0 = MFMA32(w0, hv, f0); f1 = MFMA32(w1, hv, f1); }
            });
            STAMP(9);
            float fs[8], ss[8];
            auto reduce = [&](const f32x16& b0, const f32x16& b1, float (&res)[8]) {
                bar_lds();
                const unsigned wb = L + RED + (tb * 4 + kq) * 8192 + lane * 16;
#pragma unroll
                for (int q = 0; q < 4; ++q) { *(LAS f32x4*)(uintptr_t)(wb + q * 1024) = (f32x4){b0[4 * q], b0[4 * q + 1], b0[4 * q + 2], b0[4 * q + 3]};
                                              *(LAS f32x4*)(uintptr_t)(wb + 4096 + q * 1024) = (f32x4){b1[4 * q], b1[4 * q + 1], b1[4 * q + 2], b1[4 * q + 3]}; }
                bar_lds();
                const unsigned rbs = L + RED + tb * 32768 + kq * 2048 + lane * 16;
                const f32x4 lo = (lds_f4(rbs) + lds_f4(rbs + 8192)) + (lds_f4(rbs + 16384) + lds_f4(rbs + 24576));
                const f32x4 hi = (lds_f4(rbs + 1024) + lds_f4(rbs + 9216)) + (lds_f4(rbs + 17408) + lds_f4(rbs + 25600));
#pragma unroll
                for (int e = 0; e < 4; ++e) { res[e] = lo[e]; res[4 + e] = hi[e]; } };
            reduce(f0, f1, fs);
            if (SYNC) { if (w == 0) wait_flags(ws, fl(2, 0, 0) + (rbD * 16 + (lane & 15)) * 8, tag); bar_lds(); }
            STAMP(10);
            u32x4 of[8];
#pragma unroll
            for (int i = 0; i < 8; ++i) of[i] = ld16(rW, lane * 16, OF_OFF + (T * 32 + 8 * kq + i) * 1024);
            f32x16 s0 = {}, s1 = {};
            const unsigned oa = L + WOB + 28 * kq * 512 + hh * 512 + troff;
#pragma unroll
            for (int i = 0; i < 8; ++i) { const unsigned a = oa + (7 * (i >> 1) + 4 * (i & 1)) * 512;
                const bf16x8 w0 = lds_tr2(a, a + 1024), w1 = lds_tr2(a + 256, a + 1280); const bf16x8 ov = __builtin_bit_cast(bf16x8, of[i]);
                s0 = MFMA32(w0, ov, s0); s1 = MFMA32(w1, ov, s1); }
            reduce(s0, s1, ss);
            float r[8];
#pragma unroll
            for (int e = 0; e < 8; ++e) { const float fb = bfr(fs[e]);
                const float g = bfr(fb * __builtin_amdgcn_rcpf(1.0f + __builtin_amdgcn_exp2f(fb * -1.4426950408889634f)));
                const unsigned xd = e < 4 ? xv0[(e >> 1) & 1] : xv1[(e >> 1) & 1];
                r[e] = ((e & 1) ? bhi(xd) : blo(xd)) + bfr(bfr(ss[e]) * g); }
            __builtin_amdgcn_raw_buffer_store_b64((u32x2){pk(r[0], r[1]), pk(r[2], r[3])}, rOut, xo, 0, 0);
            __builtin_amdgcn_raw_buffer_store_b64((u32x2){pk(r[4], r[5]), pk(r[6], r[7])}, rOut, xo + 16, 0, 0);
            STAMP(11);
        }
        if (SYNC && blockIdx.x == 255 && w == 0) {
#pragma unroll
            for (int i = 0; i < 4; ++i) wait_flags(ws, fl(0, 0, 0) + (i * 64 + lane) * 8, tag);
            if (lane == 0) set_flag(ws, EP_OFF, tag);
        }
    }
}

#define NLAUNCH 1
extern "C" void kernel_launch(void* const* d_in, const int* in_sizes, int n_in, void* d_out, int out_size, void* d_ws, size_t ws_size, hipStream_t stream) {
    const void *x = d_in[0], *wqkv = d_in[1], *wo = d_in[2], *wg = d_in[4], *wu = d_in[5], *wd = d_in[6];
    (void)hipFuncSetAttribute((const void*)fused_kernel<7, true>, hipFuncAttributeMaxDynamicSharedMemorySize, LDS_BYTES);
    fused_kernel<7, true><<<256, 512, LDS_BYTES, stream>>>(x, wqkv, wo, wg, wu, wd, d_out, (char*)d_ws);
}
```

```cpp
#include <hip/hip_runtime.h>
#include <stdint.h>

constexpr unsigned KC_OFF = 56 * 32768, QC_OFF = 0, VT_OFF = 0x400000, HF_OFF = 0x600000, OF_OFF = 0xD00000, FL_OFF = 0xF00000,
                   EP_OFF = FL_OFF + 0x10000, ST_OFF = FL_OFF + 0x14000, WS_BYTES = 0x1000000;
#define VLAYOUT 4
constexpr int LDS_BYTES = 163840;

__global__ __launch_bounds__(512, 2) void fused_kernel(const void* x, unsigned xn, unsigned xf, const void* wg, unsigned gn, unsigned gf, const void* wu, unsigned un, unsigned uf,
        const void* wqkv, unsigned qn, unsigned qf, void* ws, unsigned sn, unsigned sf, const void* wd, unsigned dn, unsigned df, const void* wo, const void* wg2, void* out, const void* wu2) {
    unsigned tid = threadIdx.x;
    asm volatile(
    "s_load_dwordx16 s[4:19], %1, 0\n"
    "s_load_dwordx16 s[20:35], %1, 64\n"
    "s_lshr_b32 s43, %2, 3\n"
    "v_lshrrev_b32 v77, 5, v0\n"
    "s_getpc_b64 s[38:39]\n"
    ".Lk_pc:\n"
    "v_lshl_add_u32 v77, s43, 4, v77\n"
    "v_lshlrev_b32 v77, 7, v77\n"
    "v_min_u32 v77, .Lk_end-.Lk_pc-4, v77\n"
    "global_load_dword v76, v77, s[38:39]\n"
    "s_and_b32 s46, s43, 3\n"
    "v_lshrrev_b32 v1, 6, v0\n"
    "v_and_b32 v64, 63, v0\n"
    "s_and_b32 s44, %2, 7\n"
    "s_and_b32 s45, s44, 3\n"
    "s_lshl2_add_u32 s41, s45, s46\n"
    "v_readfirstlane_b32 s40, v1\n"
    "s_movk_i32 s51, 0xe00\n"
    "s_sub_u32 s55, s41, s45\n"
    "s_add_u32 s53, s55, 9\n"
    "s_lshl1_add_u32 s47, s45, 1\n"
    "s_add_u32 s48, s47, 1\n"
    "s_cmp_eq_u32 s46, 3\n"
    "s_cselect_b32 s51, 0xa80, s51\n"
    "s_cselect_b32 s53, s47, s53\n"
    "s_cselect_b32 s52, s48, 0\n"
    "s_cselect_b32 s48, s48, s55\n"
    "s_lshr_b32 s45, s44, 2\n"
    "s_lshr_b32 s46, s43, 2\n"
    "s_lshl3_add_u32 s42, s45, s46\n"
    "v_bfe_u32 v65, v64, 2, 2\n"
    "v_lshrrev_b32 v67, 5, v64\n"
    "v_bfe_u32 v66, v64, 4, 1\n"
    "v_and_b32 v3, 3, v64\n"
    "v_lshl_add_u32 v249, v67, 2, v65\n"
    "v_lshlrev_b32 v7, 4, v3\n"
    "v_lshl_add_u32 v4, s40, 3, v249\n"
    "v_lshl_add_u32 v69, v66, 6, v7\n"
    "v_lshl_add_u32 v5, s48, 7, v69\n"
    "v_lshl_add_u32 v6, s53, 7, v69\n"
    "s_movk_i32 s48, 0xa80\n"
    "v_mad_u32_u24 v79, v4, s51, v5\n"
    "v_mad_u32_u24 v20, v4, s48, v6\n"
    "s_lshl3_add_u32 s45, s42, s40\n"
    "s_mul_i32 s45, s45, 0x3800\n"
    "v_lshrrev_b32 v1, 3, v64\n"
    "v_lshrrev_b32 v2, 4, v64\n"
    "v_and_b32 v8, 7, v64\n"
    "v_xor_b32 v8, v8, v2\n"
    "v_mul_u32_u24 v1, 0x380, v1\n"
    "v_lshl_add_u32 v1, v8, 4, v1\n"
    "s_add_u32 s47, s45, 0x1800\n"
    "v_xor_b32 v2, 64, v1\n"
    "v_add_u32 v18, s45, v1\n"
    "v_add_u32 v19, s47, v2\n"
    "s_lshl_b32 s49, s40, 11\n"
    "s_lshl_b32 s50, s40, 10\n"
    "s_lshl_b32 s51, s51, 6\n"
    "s_add_u32 s45, s43, s46\n"
    "s_bitcmp1_b32 s45, 0\n"
    "s_mov_b32 s72, 0x6543210\n"
    "s_cselect_b32 s72, 0x3654021, s72\n"
    "s_mov_b32 s46, 0x4365102\n"
    "s_cselect_b32 s46, 0x5436210, s46\n"
    "s_bitcmp1_b32 s45, 1\n"
    "s_cselect_b32 s72, s46, s72\n"
    "s_bfe_u32 s48, s72, 0x30000\n"
    "s_add_u32 m0, s49, 0x4000\n"
    "s_lshl_b32 s45, s48, 7\n"
    "s_mul_i32 s47, s48, s51\n"
    "s_waitcnt lgkmcnt(0)\n"
    "buffer_load_dwordx4 v18, s[4:7], s45 offen lds\n"
    "s_cmp_lg_u32 s52, 0\n"
    "s_cselect_b64 s[8:9], s[16:17], s[8:9]\n"
    "s_cselect_b32 s10, 0x126000, s10\n"
    "buffer_load_dwordx4 v19, s[4:7], s45 offen offset:1024 lds\n"
    "s_cselect_b64 s[12:13], s[16:17], s[12:13]\n"
    "s_mov_b32 s14, s10\n"
    "s_sub_u32 s16, s16, 0x400\n"
    "s_subb_u32 s17, s17, 0\n"
    "s_add_u32 m0, s49, 0x10000\n"
    "s_mul_i32 s48, s48, 0x2a000\n"
    "buffer_load_dwordx4 v79, s[8:11], s47 offen lds\n"
    "buffer_load_dwordx4 v20, s[16:19], s48 offen offset:1024 lds\n"
    "s_and_b32 s70, s40, 3\n"
    "s_lshr_b32 s38, s40, 2\n"
    "s_lshl_b32 s39, s38, 1\n"
    "s_xor_b32 s39, s39, s70\n"
    "v_and_b32 v68, 31, v64\n"
    "v_lshlrev_b32 v10, 6, v65\n"
    "v_bfe_u32 v1, v64, 1, 3\n"
    "v_lshl_add_u32 v5, s39, 1, v67\n"
    "v_lshl_add_u32 v10, v66, 5, v10\n"
    "v_xor_b32 v1, v1, v5\n"
    "v_lshl_add_u32 v7, s70, 5, v68\n"
    "v_lshl_add_u32 v10, v3, 3, v10\n"
    "v_lshlrev_b32 v1, 4, v1\n"
    "v_lshl_add_u32 v6, s38, 8, v10\n"
    "v_lshl_add_u32 v48, v7, 7, v1\n"
    "v_lshl_add_u32 v8, v5, 11, v6\n"
    "v_xor_b32 v49, 32, v48\n"
    "v_xor_b32 v50, 64, v48\n"
    "v_xor_b32 v51, 0x60, v48\n"
    "v_add_u32 v52, 0xc000, v8\n"
    "v_bfe_u32 v1, v64, 2, 1\n"
    "v_xor_b32 v1, v1, v66\n"
    "v_or_b32 v1, v1, v3\n"
    "v_xor_b32 v53, 0x1000, v52\n"
    "v_xor_b32 v54, 0x2000, v52\n"
    "v_xor_b32 v55, 0x3000, v52\n"
    "v_cmp_eq_u32 vcc, 0, v1\n"
    "v_mov_b32 v2, 0x3f803f80\n"
    "s_mov_b32 s71, 0xffff0000\n"
    "v_mov_b32 v56, 0\n"
    "v_mov_b32 v57, 0\n"
    "v_mov_b32 v58, 0\n"
    "v_mov_b32 v59, 0\n"
    "v_cndmask_b32 v116, 0, v2, vcc\n"
    "v_mov_b32 v117, v116\n"
    "v_mov_b32 v118, v116\n"
    "v_mov_b32 v119, v116\n"
    "v_lshl_add_u32 v8, v5, 10, v6\n"
    "v_add_u32 v124, 0x18000, v8\n"
    "v_xor_b32 v125, 0x800, v124\n"
    "v_xor_b32 v126, 0x1000, v124\n"
    "v_xor_b32 v127, 0x1800, v124\n"
    "s_lshl_b32 s56, s52, 7\n"
    "v_subrev_u32 v21, s56, v79\n"
    "s_lshr_b32 s56, s41, 2\n"
    "s_add_u32 s56, s56, 12\n"
    "s_cmp_eq_u32 s52, 0\n"
    "s_cselect_b32 s55, s55, s56\n"
    "s_add_u32 s55, s55, 12\n"
    "v_lshl_add_u32 v1, s55, 7, v69\n"
    "s_movk_i32 s56, 0xe00\n"
    "v_mad_u32_u24 v78, v4, s56, v1\n"
    "s_lshl2_add_u32 s54, s42, s70\n"
    "v_lshlrev_b32 v244, 4, v64\n"
    "s_lshl1_add_u32 s56, s53, s38\n"
    "s_lshl_b32 s56, s56, 8\n"
    "s_add_u32 s56, s56, s54\n"
    "s_lshl_b32 s56, s56, 9\n"
    "s_lshl_b32 s58, s54, 12\n"
    "s_add_u32 s58, s58, 0x400000\n"
    "s_cmp_ge_u32 s53, 14\n"
    "s_cselect_b32 s56, s58, s56\n"
    "v_lshlrev_b32 v17, 5, v7\n"
    "v_lshl_add_u32 v1, s38, 1, v67\n"
    "v_add_u32 v17, 0x26000, v17\n"
    "v_lshl_add_u32 v4, s38, 5, v68\n"
    "v_lshl_add_u32 v16, v1, 3, v17\n"
    "v_lshlrev_b32 v248, 4, v4\n"
    "v_add_u32 v248, 0x27000, v248\n"
    "v_lshl_add_u32 v247, s70, 2, v248\n"
    "s_add_u32 s58, s53, 2\n"
    "s_and_b32 s58, s58, 7\n"
    "v_lshl_add_u32 v4, s58, 6, v4\n"
    "v_mul_u32_u24 v5, 0x925, v4\n"
    "v_lshlrev_b32 v1, 6, v68\n"
    "v_lshrrev_b32 v5, 17, v5\n"
    "v_lshl_add_u32 v1, v67, 3, v1\n"
    "v_mul_u32_u24 v5, 0x3fc8, v5\n"
    "v_add_u32 v243, s49, v1\n"
    "v_lshl_add_u32 v4, v67, 6, v4\n"
    "v_lshl_add_u32 v1, v67, 8, v10\n"
    "v_add_lshl_u32 v242, v4, v5, 4\n"
    "v_add_u32 v246, s49, v1\n"
    "v_lshl_add_u32 v1, v67, 11, v68\n"
    "v_lshlrev_b32 v245, 4, v1\n"
    "s_lshl4_add_u32 s58, s41, s42\n"
    "s_lshl_b32 s58, s58, 7\n"
    "s_add_u32 s58, s58, 0xf00000\n"
    "s_mul_i32 s70, s43, 37\n"
    "s_lshr_b32 s70, s70, 8\n"
    "s_mul_i32 s38, s70, 7\n"
    "s_sub_u32 s38, s43, s38\n"
    "s_add_u32 s73, s70, s38\n"
    "s_and_b32 s73, s73, 3\n"
    "s_lshl_b32 s38, s38, 7\n"
    "s_mul_i32 s60, s40, 0x1c00\n"
    "s_add_u32 s60, s60, s38\n"
    "s_movk_i32 s39, 0x380\n"
    "v_mad_u32_u24 v249, v249, s39, v69\n"
    "v_add_u32 v249, s60, v249\n"
    "s_cmp_lt_u32 s43, 28\n"
    "s_cselect_b32 s26, s26, 0\n"
    "s_min_u32 s70, s70, 3\n"
    "s_lshl2_add_u32 s70, s44, s70\n"
    "s_and_b32 s39, s40, 1\n"
    "s_lshl1_add_u32 s70, s70, s39\n"
    "s_lshr_b32 s39, s40, 1\n"
    "s_lshl2_add_u32 s68, s70, s39\n"
    "s_lshl_b32 s68, s68, 13\n"
    "s_add_u32 s68, s68, 0xd00000\n"
    "s_mul_i32 s64, s70, 0x70\n"
    "s_add_u32 s64, s64, s39\n"
    "s_lshl2_add_u32 s64, s73, s64\n"
    "s_lshl_b32 s64, s64, 10\n"
    "s_add_u32 s64, s64, 0x600000\n"
    "s_mul_i32 s69, s70, 0x7000\n"
    "s_add_u32 s69, s69, s38\n"
    "s_lshl_b32 s38, s39, 5\n"
    "s_add_u32 s69, s69, s38\n"
    "s_lshr_b32 s38, s40, 2\n"
    "s_lshl1_add_u32 s66, s70, s38\n"
    "s_lshl_b32 s66, s66, 7\n"
    "s_add_u32 s66, s66, 0xf40000\n"
    "s_mul_i32 s57, s54, 56\n"
    "s_lshl1_add_u32 s57, s55, s57\n"
    "s_add_u32 s57, s57, s38\n"
    "s_lshl_b32 s57, s57, 11\n"
    "s_add_u32 s57, s57, 0x600000\n"
    "s_bfe_u32 s48, s72, 0x30004\n"
    "s_add_u32 m0, s49, 0x8000\n"
    "s_lshl_b32 s45, s48, 7\n"
    "buffer_load_dwordx4 v18, s[4:7], s45 offen lds\n"
    "s_mul_i32 s47, s48, s51\n"
    "buffer_load_dwordx4 v19, s[4:7], s45 offen offset:1024 lds\n"
    "s_add_u32 m0, s49, 0x14000\n"
    "s_mul_i32 s48, s48, 0x2a000\n"
    "buffer_load_dwordx4 v79, s[8:11], s47 offen lds\n"
    "buffer_load_dwordx4 v20, s[16:19], s48 offen offset:1024 lds\n"
    "v_xor_b32 v70, s39, v66\n"
    "v_and_b32 v71, 1, v64\n"
    "v_and_b32 v70, 1, v70\n"
    "v_bfe_u32 v72, v64, 1, 1\n"
    "v_lshlrev_b32 v70, 5, v70\n"
    "s_lshl3_add_u32 s39, s39, s38\n"
    "s_lshl_b32 s39, s39, 8\n"
    "v_lshl_add_u32 v71, v71, 4, s39\n"
    "v_lshl_add_u32 v70, v65, 6, v70\n"
    "v_lshl_add_u32 v71, v72, 3, v71\n"
    "v_lshl_add_u32 v70, v67, 9, v70\n"
    "v_add_u32 v251, v70, v71\n"
    "v_xor_b32 v70, s38, v66\n"
    "v_and_b32 v71, 3, v64\n"
    "v_xor_b32 v252, 0x100, v251\n"
    "v_mad_u32_u24 v70, v70, 14, v71\n"
    "v_and_b32 v71, 1, v65\n"
    "v_lshl_add_u32 v70, v65, 1, v70\n"
    "v_mad_u32_u24 v70, v71, 5, v70\n"
    "v_lshl_add_u32 v70, v67, 6, v70\n"
    "v_lshlrev_b32 v254, 4, v70\n"
    "v_add_u32 v255, s49, v244\n"
    "s_lshl1_add_u32 s59, s54, s38\n"
    "s_lshl4_add_u32 s59, s59, s41\n"
    "s_lshl_b32 s59, s59, 3\n"
    "s_add_u32 s59, s59, 0xf40000\n"
    "s_and_b32 s41, s43, 1\n"
    "s_lshl1_add_u32 s41, s44, s41\n"
    "s_lshr_b32 s42, s43, 1\n"
    "s_and_b32 s54, s40, 3\n"
    "s_lshl2_add_u32 s54, s42, s54\n"
    "s_lshl4_add_u32 s67, s54, s41\n"
    "s_lshl1_add_u32 s67, s67, s38\n"
    "s_lshl_b32 s67, s67, 10\n"
    "s_add_u32 s67, s67, 0xd00000\n"
    "s_mul_i32 s70, s41, 7\n"
    "s_lshr_b32 s70, s70, 1\n"
    "s_lshl1_add_u32 s65, s38, s70\n"
    "s_add_u32 s70, s70, 56\n"
    "v_and_b32 v71, 1, v64\n"
    "v_mad_u32_u24 v70, v65, 56, s70\n"
    "v_add_u32 v73, s65, v71\n"
    "v_mad_u32_u24 v70, v72, 3, v70\n"
    "v_lshl_add_u32 v71, s40, 1, v71\n"
    "v_mov_b32 v72, s42\n"
    "v_cmp_le_u32 vcc, 8, v64\n"
    "s_nop 1\n"
    "v_cndmask_b32 v70, v70, v73, vcc\n"
    "v_cndmask_b32 v71, v71, v72, vcc\n"
    "v_lshrrev_b32 v72, 3, v70\n"
    "v_subrev_u32 v73, 8, v70\n"
    "v_subrev_u32 v72, 9, v72\n"
    "v_lshrrev_b32 v73, 2, v73\n"
    "v_mul_u32_u24 v74, 11, v72\n"
    "v_cmp_gt_u32 vcc, 8, v70\n"
    "s_nop 1\n"
    "v_cndmask_b32 v73, v73, v70, vcc\n"
    "v_lshrrev_b32 v74, 5, v74\n"
    "v_or_b32 v73, 3, v73\n"
    "v_add_u32 v72, v72, v74\n"
    "v_cmp_lt_u32 vcc, 0x47, v70\n"
    "s_nop 1\n"
    "v_cndmask_b32 v72, v73, v72, vcc\n"
    "v_and_b32 v73, 15, v64\n"
    "v_lshl_add_u32 v72, v72, 4, v71\n"
    "v_lshlrev_b32 v253, 3, v73\n"
    "v_lshlrev_b32 v250, 7, v72\n"
    "s_lshl_b32 s65, s65, 6\n"
    "s_add_u32 s65, s65, s54\n"
    "s_lshl_b32 s65, s65, 9\n"
    "s_and_b32 s39, s42, 7\n"
    "s_lshl3_add_u32 s39, s40, s39\n"
    "s_lshl_b32 s61, s70, 6\n"
    "s_add_u32 s61, s61, s39\n"
    "s_lshl_b32 s61, s61, 9\n"
    "s_add_u32 s62, s61, 0x10000\n"
    "s_lshr_b32 s63, s41, 1\n"
    "s_lshl_b32 s63, s63, 6\n"
    "s_add_u32 s63, s63, s39\n"
    "s_lshl_b32 s63, s63, 12\n"
    "s_bitcmp1_b32 s41, 0\n"
    "s_cselect_b32 s39, 0x1c0, 0\n"
    "s_add_u32 s63, s63, 0x400000\n"
    "s_add_u32 s63, s63, s39\n"
    "s_mov_b32 s46, 0xf10000\n"
    "buffer_load_dwordx2 v[240:241], off, s[20:23], s46 sc1\n"
    "s_waitcnt vmcnt(5)\n"
    "s_barrier\n"
    "ds_read_b64_tr_b16 v[80:81], v52 offset:17408\n"
    "ds_read_b64_tr_b16 v[82:83], v52 offset:17920\n"
    "ds_read_b128 v[128:131], v48 offset:16384\n"
    "ds_read_b64_tr_b16 v[84:85], v52 offset:16384\n"
    "ds_read_b64_tr_b16 v[86:87], v52 offset:16896\n"
    "v_mov_b32 v100, 0x3f803f80\n"
    "v_mov_b32 v101, 0x3f803f80\n"
    "s_setprio 2\n"
    "s_waitcnt lgkmcnt(2)\n"
    "v_mfma_f32_32x32x16_bf16 v[32:47], v[80:83], v[128:131], 0\n"
    "s_bfe_u32 s48, s72, 0x30008\n"
    "s_add_u32 m0, s49, 0\n"
    "ds_read_b64_tr_b16 v[88:89], v53 offset:17408\n"
    "ds_read_b64_tr_b16 v[90:91], v53 offset:17920\n"
    "s_lshl_b32 s45, s48, 7\n"
    "buffer_load_dwordx4 v18, s[4:7], s45 offen lds\n"
    "v_lshlrev_b32 v60, 16, v128\n"
    "v_and_b32 v61, s71, v128\n"
    "v_add_f32 v56, v56, v60\n"
    "v_add_f32 v58, v58, v61\n"
    "v_fmac_f32 v57, v60, v60\n"
    "v_mfma_f32_16x16x32_bf16 v[104:107], v[116:119], v[80:83], 0\n"
    "ds_read_b128 v[132:135], v49 offset:16384\n"
    "v_fmac_f32 v59, v61, v61\n"
    "v_lshlrev_b32 v62, 16, v129\n"
    "v_and_b32 v63, s71, v129\n"
    "s_waitcnt lgkmcnt(3)\n"
    "v_mfma_f32_32x32x16_bf16 v[0:15], v[84:87], v[128:131], 0\n"
    "s_mul_i32 s47, s48, s51\n"
    "buffer_load_dwordx4 v19, s[4:7], s45 offen offset:1024 lds\n"
    "ds_read_b64_tr_b16 v[92:93], v53 offset:16384\n"
    "v_add_f32 v56, v56, v62\n"
    "v_add_f32 v58, v58, v63\n"
    "v_fmac_f32 v57, v62, v62\n"
    "v_fmac_f32 v59, v63, v63\n"
    "v_lshlrev_b32 v60, 16, v130\n"
    "v_mfma_f32_16x16x32_bf16 v[108:111], v[116:119], v[84:87], 0\n"
    "ds_read_b64_tr_b16 v[94:95], v53 offset:16896\n"
    "v_and_b32 v61, s71, v130\n"
    "v_add_f32 v56, v56, v60\n"
    "v_add_f32 v58, v58, v61\n"
    "s_setprio 1\n"
    "s_waitcnt lgkmcnt(2)\n"
    "v_mfma_f32_32x32x16_bf16 v[32:47], v[88:91], v[132:135], v[32:47]\n"
    "s_add_u32 m0, s49, 0xc000\n"
    "s_mul_i32 s48, s48, 0x2a000\n"
    "ds_read_b64_tr_b16 v[80:81], v54 offset:17408\n"
    "ds_read_b64_tr_b16 v[82:83], v54 offset:17920\n"
    "ds_read_b128 v[136:139], v50 offset:16384\n"
    "buffer_load_dwordx4 v79, s[8:11], s47 offen lds\n"
    "v_fmac_f32 v57, v60, v60\n"
    "v_fmac_f32 v59, v61, v61\n"
    "v_lshlrev_b32 v62, 16, v131\n"
    "v_and_b32 v63, s71, v131\n"
    "v_add_f32 v56, v56, v62\n"
    "v_mfma_f32_4x4x4_16b_bf16 v[24:27], v[132:133], v[132:133], 0\n"
    "v_mfma_f32_4x4x4_16b_bf16 v[28:31], v[100:101], v[132:133], 0\n"
    "s_waitcnt lgkmcnt(3)\n"
    "v_mfma_f32_32x32x16_bf16 v[0:15], v[92:95], v[132:135], v[0:15]\n"
    "ds_read_b64_tr_b16 v[84:85], v54 offset:16384\n"
    "ds_read_b64_tr_b16 v[86:87], v54 offset:16896\n"
    "v_add_f32 v58, v58, v63\n"
    "v_fmac_f32 v57, v62, v62\n"
    "v_fmac_f32 v59, v63, v63\n"
    "v_mfma_f32_4x4x4_16b_bf16 v[24:27], v[134:135], v[134:135], v[24:27]\n"
    "v_mfma_f32_4x4x4_16b_bf16 v[28:31], v[100:101], v[134:135], v[28:31]\n"
    "s_setprio 0\n"
    "s_waitcnt lgkmcnt(2)\n"
    "v_mfma_f32_32x32x16_bf16 v[32:47], v[80:83], v[136:139], v[32:47]\n"
    "buffer_load_dwordx4 v20, s[16:19], s48 offen offset:1024 lds\n"
    "ds_read_b64_tr_b16 v[88:89], v55 offset:17408\n"
    "ds_read_b64_tr_b16 v[90:91], v55 offset:17920\n"
    "ds_read_b128 v[140:143], v51 offset:16384\n"
    "ds_read_b64_tr_b16 v[92:93], v55 offset:16384\n"
    "ds_read_b64_tr_b16 v[94:95], v55 offset:16896\n"
    "s_waitcnt lgkmcnt(5)\n"
    "v_mfma_f32_32x32x16_bf16 v[0:15], v[84:87], v[136:139], v[0:15]\n"
    "s_waitcnt vmcnt(5)\n"
    "s_barrier\n"
    "s_setprio 3\n"
    "s_waitcnt lgkmcnt(2)\n"
    "v_mfma_f32_32x32x16_bf16 v[32:47], v[88:91], v[140:143], v[32:47]\n"
    "s_bfe_u32 s48, s72, 0x3000c\n"
    "s_add_u32 m0, s49, 0x1c000\n"
    "ds_read_b64_tr_b16 v[80:81], v52 offset:33792\n"
    "ds_read_b64_tr_b16 v[82:83], v52 offset:34304\n"
    "ds_read_b128 v[144:147], v48 offset:32768\n"
    "s_lshl_b32 s45, s48, 7\n"
    "buffer_load_dwordx4 v18, s[4:7], s45 offen lds\n"
    "s_waitcnt lgkmcnt(3)\n"
    "v_mfma_f32_32x32x16_bf16 v[0:15], v[92:95], v[140:143], v[0:15]\n"
    "ds_read_b64_tr_b16 v[84:85], v52 offset:32768\n"
    "ds_read_b64_tr_b16 v[86:87], v52 offset:33280\n"
    "s_setprio 2\n"
    "s_waitcnt lgkmcnt(2)\n"
    "v_mfma_f32_32x32x16_bf16 v[32:47], v[80:83], v[144:147], v[32:47]\n"
    "s_mul_i32 s47, s48, s51\n"
    "buffer_load_dwordx4 v19, s[4:7], s45 offen offset:1024 lds\n"
    "ds_read_b64_tr_b16 v[88:89], v53 offset:33792\n"
    "ds_read_b64_tr_b16 v[90:91], v53 offset:34304\n"
    "v_lshlrev_b32 v60, 16, v144\n"
    "v_and_b32 v61, s71, v144\n"
    "v_add_f32 v56, v56, v60\n"
    "v_add_f32 v58, v58, v61\n"
    "v_fmac_f32 v57, v60, v60\n"
    "v_mfma_f32_16x16x32_bf16 v[104:107], v[116:119], v[80:83], v[104:107]\n"
    "ds_read_b128 v[148:151], v49 offset:32768\n"
    "v_fmac_f32 v59, v61, v61\n"
    "v_lshlrev_b32 v62, 16, v145\n"
    "v_and_b32 v63, s71, v145\n"
    "s_waitcnt lgkmcnt(3)\n"
    "v_mfma_f32_32x32x16_bf16 v[0:15], v[84:87], v[144:147], v[0:15]\n"
    "s_add_u32 m0, s49, 0x18000\n"
    "s_mul_i32 s48, s48, 0x2a000\n"
    "ds_read_b64_tr_b16 v[92:93], v53 offset:32768\n"
    "buffer_load_dwordx4 v79, s[8:11], s47 offen lds\n"
    "v_add_f32 v56, v56, v62\n"
    "v_add_f32 v58, v58, v63\n"
    "v_fmac_f32 v57, v62, v62\n"
    "v_fmac_f32 v59, v63, v63\n"
    "v_lshlrev_b32 v60, 16, v146\n"
    "v_mfma_f32_16x16x32_bf16 v[108:111], v[116:119], v[84:87], v[108:111]\n"
    "ds_read_b64_tr_b16 v[94:95], v53 offset:33280\n"
    "v_and_b32 v61, s71, v146\n"
    "v_add_f32 v56, v56, v60\n"
    "v_add_f32 v58, v58, v61\n"
    "s_setprio 1\n"
    "s_waitcnt lgkmcnt(2)\n"
    "v_mfma_f32_32x32x16_bf16 v[32:47], v[88:91], v[148:151], v[32:47]\n"
    "buffer_load_dwordx4 v20, s[16:19], s48 offen offset:1024 lds\n"
    "ds_read_b64_tr_b16 v[80:81], v54 offset:33792\n"
    "ds_read_b64_tr_b16 v[82:83], v54 offset:34304\n"
    "ds_read_b128 v[152:155], v50 offset:32768\n"
    "v_fmac_f32 v57, v60, v60\n"
    "v_fmac_f32 v59, v61, v61\n"
    "v_lshlrev_b32 v62, 16, v147\n"
    "v_and_b32 v63, s71, v147\n"
    "v_add_f32 v56, v56, v62\n"
    "v_mfma_f32_4x4x4_16b_bf16 v[24:27], v[148:149], v[148:149], v[24:27]\n"
    "v_mfma_f32_4x4x4_16b_bf16 v[28:31], v[100:101], v[148:149], v[28:31]\n"
    "s_waitcnt lgkmcnt(3)\n"
    "v_mfma_f32_32x32x16_bf16 v[0:15], v[92:95], v[148:151], v[0:15]\n"
    "ds_read_b64_tr_b16 v[84:85], v54 offset:32768\n"
    "ds_read_b64_tr_b16 v[86:87], v54 offset:33280\n"
    "v_add_f32 v58, v58, v63\n"
    "v_fmac_f32 v57, v62, v62\n"
    "v_fmac_f32 v59, v63, v63\n"
    "v_mfma_f32_4x4x4_16b_bf16 v[24:27], v[150:151], v[150:151], v[24:27]\n"
    "v_mfma_f32_4x4x4_16b_bf16 v[28:31], v[100:101], v[150:151], v[28:31]\n"
    "s_setprio 0\n"
    "s_waitcnt lgkmcnt(2)\n"
    "v_mfma_f32_32x32x16_bf16 v[32:47], v[80:83], v[152:155], v[32:47]\n"
    "s_bfe_u32 s46, s72, 0x30000\n"
    "s_add_u32 m0, s50, 0x20000\n"
    "ds_read_b64_tr_b16 v[88:89], v55 offset:33792\n"
    "ds_read_b64_tr_b16 v[90:91], v55 offset:34304\n"
    "ds_read_b128 v[156:159], v51 offset:32768\n"
    "ds_read_b64_tr_b16 v[92:93], v55 offset:32768\n"
    "ds_read_b64_tr_b16 v[94:95], v55 offset:33280\n"
    "s_mul_i32 s46, s46, s51\n"
    "buffer_load_dwordx4 v21, s[12:15], s46 offen lds\n"
    "s_waitcnt lgkmcnt(5)\n"
    "v_mfma_f32_32x32x16_bf16 v[0:15], v[84:87], v[152:155], v[0:15]\n"
    "s_waitcnt vmcnt(5)\n"
    "s_barrier\n"
    "s_setprio 3\n"
    "s_waitcnt lgkmcnt(2)\n"
    "v_mfma_f32_32x32x16_bf16 v[32:47], v[88:91], v[156:159], v[32:47]\n"
    "s_bfe_u32 s48, s72, 0x30010\n"
    "s_add_u32 m0, s49, 0x4000\n"
    "ds_read_b64_tr_b16 v[80:81], v52 offset:1024\n"
    "ds_read_b64_tr_b16 v[82:83], v52 offset:1536\n"
    "ds_read_b128 v[160:163], v48\n"
    "s_lshl_b32 s45, s48, 7\n"
    "buffer_load_dwordx4 v18, s[4:7], s45 offen lds\n"
    "s_waitcnt lgkmcnt(3)\n"
    "v_mfma_f32_32x32x16_bf16 v[0:15], v[92:95], v[156:159], v[0:15]\n"
    "ds_read_b64_tr_b16 v[84:85], v52\n"
    "ds_read_b64_tr_b16 v[86:87], v52 offset:512\n"
    "s_setprio 2\n"
    "s_waitcnt lgkmcnt(2)\n"
    "v_mfma_f32_32x32x16_bf16 v[32:47], v[80:83], v[160:163], v[32:47]\n"
    "s_mul_i32 s47, s48, s51\n"
    "buffer_load_dwordx4 v19, s[4:7], s45 offen offset:1024 lds\n"
    "ds_read_b64_tr_b16 v[88:89], v53 offset:1024\n"
    "ds_read_b64_tr_b16 v[90:91], v53 offset:1536\n"
    "v_lshlrev_b32 v60, 16, v160\n"
    "v_and_b32 v61, s71, v160\n"
    "v_add_f32 v56, v56, v60\n"
    "v_add_f32 v58, v58, v61\n"
    "v_fmac_f32 v57, v60, v60\n"
    "v_mfma_f32_16x16x32_bf16 v[104:107], v[116:119], v[80:83], v[104:107]\n"
    "ds_read_b128 v[164:167], v49\n"
    "v_fmac_f32 v59, v61, v61\n"
    "v_lshlrev_b32 v62, 16, v161\n"
    "v_and_b32 v63, s71, v161\n"
    "s_waitcnt lgkmcnt(3)\n"
    "v_mfma_f32_32x32x16_bf16 v[0:15], v[84:87], v[160:163], v[0:15]\n"
    "s_add_u32 m0, s49, 0x10000\n"
    "s_mul_i32 s48, s48, 0x2a000\n"
    "ds_read_b64_tr_b16 v[92:93], v53\n"
    "buffer_load_dwordx4 v79, s[8:11], s47 offen lds\n"
    "v_add_f32 v56, v56, v62\n"
    "v_add_f32 v58, v58, v63\n"
    "v_fmac_f32 v57, v62, v62\n"
    "v_fmac_f32 v59, v63, v63\n"
    "v_lshlrev_b32 v60, 16, v162\n"
    "v_mfma_f32_16x16x32_bf16 v[108:111], v[116:119], v[84:87], v[108:111]\n"
    "ds_read_b64_tr_b16 v[94:95], v53 offset:512\n"
    "v_and_b32 v61, s71, v162\n"
    "v_add_f32 v56, v56, v60\n"
    "v_add_f32 v58, v58, v61\n"
    "s_setprio 1\n"
    "s_waitcnt lgkmcnt(2)\n"
    "v_mfma_f32_32x32x16_bf16 v[32:47], v[88:91], v[164:167], v[32:47]\n"
    "buffer_load_dwordx4 v20, s[16:19], s48 offen offset:1024 lds\n"
    "ds_read_b64_tr_b16 v[80:81], v54 offset:1024\n"
    "ds_read_b64_tr_b16 v[82:83], v54 offset:1536\n"
    "ds_read_b128 v[168:171], v50\n"
    "v_fmac_f32 v57, v60, v60\n"
    "v_fmac_f32 v59, v61, v61\n"
    "v_lshlrev_b32 v62, 16, v163\n"
    "v_and_b32 v63, s71, v163\n"
    "v_add_f32 v56, v56, v62\n"
    "v_mfma_f32_4x4x4_16b_bf16 v[24:27], v[164:165], v[164:165], v[24:27]\n"
    "v_mfma_f32_4x4x4_16b_bf16 v[28:31], v[100:101], v[164:165], v[28:31]\n"
    "s_waitcnt lgkmcnt(3)\n"
    "v_mfma_f32_32x32x16_bf16 v[0:15], v[92:95], v[164:167], v[0:15]\n"
    "ds_read_b64_tr_b16 v[84:85], v54\n"
    "ds_read_b64_tr_b16 v[86:87], v54 offset:512\n"
    "v_add_f32 v58, v58, v63\n"
    "v_fmac_f32 v57, v62, v62\n"
    "v_fmac_f32 v59, v63, v63\n"
    "v_mfma_f32_4x4x4_16b_bf16 v[24:27], v[166:167], v[166:167], v[24:27]\n"
    "v_mfma_f32_4x4x4_16b_bf16 v[28:31], v[100:101], v[166:167], v[28:31]\n"
    "s_setprio 0\n"
    "s_waitcnt lgkmcnt(2)\n"
    "v_mfma_f32_32x32x16_bf16 v[32:47], v[80:83], v[168:171], v[32:47]\n"
    "s_bfe_u32 s46, s72, 0x30004\n"
    "s_add_u32 m0, s50, 0x22000\n"
    "ds_read_b64_tr_b16 v[88:89], v55 offset:1024\n"
    "ds_read_b64_tr_b16 v[90:91], v55 offset:1536\n"
    "ds_read_b128 v[172:175], v51\n"
    "ds_read_b64_tr_b16 v[92:93], v55\n"
    "ds_read_b64_tr_b16 v[94:95], v55 offset:512\n"
    "s_mul_i32 s46, s46, s51\n"
    "buffer_load_dwordx4 v21, s[12:15], s46 offen lds\n"
    "s_waitcnt lgkmcnt(5)\n"
    "v_mfma_f32_32x32x16_bf16 v[0:15], v[84:87], v[168:171], v[0:15]\n"
    "s_waitcnt vmcnt(6)\n"
    "s_barrier\n"
    "s_setprio 3\n"
    "s_waitcnt lgkmcnt(2)\n"
    "v_mfma_f32_32x32x16_bf16 v[32:47], v[88:91], v[172:175], v[32:47]\n"
    "s_bfe_u32 s48, s72, 0x30014\n"
    "s_add_u32 m0, s49, 0x8000\n"
    "ds_read_b64_tr_b16 v[80:81], v52 offset:50176\n"
    "ds_read_b64_tr_b16 v[82:83], v52 offset:50688\n"
    "v_xor_b32 v48, 0x10000, v48\n"
    "ds_read_b128 v[176:179], v48 offset:49152\n"
    "s_lshl_b32 s45, s48, 7\n"
    "buffer_load_dwordx4 v18, s[4:7], s45 offen lds\n"
    "s_waitcnt lgkmcnt(3)\n"
    "v_mfma_f32_32x32x16_bf16 v[0:15], v[92:95], v[172:175], v[0:15]\n"
    "ds_read_b64_tr_b16 v[84:85], v52 offset:49152\n"
    "ds_read_b64_tr_b16 v[86:87], v52 offset:49664\n"
    "s_setprio 2\n"
    "s_waitcnt lgkmcnt(2)\n"
    "v_mfma_f32_32x32x16_bf16 v[32:47], v[80:83], v[176:179], v[32:47]\n"
    "s_mul_i32 s47, s48, s51\n"
    "buffer_load_dwordx4 v19, s[4:7], s45 offen offset:1024 lds\n"
    "ds_read_b64_tr_b16 v[88:89], v53 offset:50176\n"
    "ds_read_b64_tr_b16 v[90:91], v53 offset:50688\n"
    "v_lshlrev_b32 v60, 16, v176\n"
    "v_and_b32 v61, s71, v176\n"
    "v_add_f32 v56, v56, v60\n"
    "v_add_f32 v58, v58, v61\n"
    "v_fmac_f32 v57, v60, v60\n"
    "v_mfma_f32_16x16x32_bf16 v[104:107], v[116:119], v[80:83], v[104:107]\n"
    "v_xor_b32 v49, 0x10000, v49\n"
    "ds_read_b128 v[180:183], v49 offset:49152\n"
    "v_fmac_f32 v59, v61, v61\n"
    "v_lshlrev_b32 v62, 16, v177\n"
    "v_and_b32 v63, s71, v177\n"
    "s_waitcnt lgkmcnt(3)\n"
    "v_mfma_f32_32x32x16_bf16 v[0:15], v[84:87], v[176:179], v[0:15]\n"
    "s_add_u32 m0, s49, 0x14000\n"
    "s_mul_i32 s48, s48, 0x2a000\n"
    "ds_read_b64_tr_b16 v[92:93], v53 offset:49152\n"
    "buffer_load_dwordx4 v79, s[8:11], s47 offen lds\n"
    "v_add_f32 v56, v56, v62\n"
    "v_add_f32 v58, v58, v63\n"
    "v_fmac_f32 v57, v62, v62\n"
    "v_fmac_f32 v59, v63, v63\n"
    "v_lshlrev_b32 v60, 16, v178\n"
    "v_mfma_f32_16x16x32_bf16 v[108:111], v[116:119], v[84:87], v[108:111]\n"
    "ds_read_b64_tr_b16 v[94:95], v53 offset:49664\n"
    "v_and_b32 v61, s71, v178\n"
    "v_add_f32 v56, v56, v60\n"
    "v_add_f32 v58, v58, v61\n"
    "s_setprio 1\n"
    "s_waitcnt lgkmcnt(2)\n"
    "v_mfma_f32_32x32x16_bf16 v[32:47], v[88:91], v[180:183], v[32:47]\n"
    "buffer_load_dwordx4 v20, s[16:19], s48 offen offset:1024 lds\n"
    "ds_read_b64_tr_b16 v[80:81], v54 offset:50176\n"
    "ds_read_b64_tr_b16 v[82:83], v54 offset:50688\n"
    "v_xor_b32 v50, 0x10000, v50\n"
    "ds_read_b128 v[184:187], v50 offset:49152\n"
    "v_fmac_f32 v57, v60, v60\n"
    "v_fmac_f32 v59, v61, v61\n"
    "v_lshlrev_b32 v62, 16, v179\n"
    "v_and_b32 v63, s71, v179\n"
    "v_add_f32 v56, v56, v62\n"
    "v_mfma_f32_4x4x4_16b_bf16 v[24:27], v[180:181], v[180:181], v[24:27]\n"
    "v_mfma_f32_4x4x4_16b_bf16 v[28:31], v[100:101], v[180:181], v[28:31]\n"
    "s_waitcnt lgkmcnt(3)\n"
    "v_mfma_f32_32x32x16_bf16 v[0:15], v[92:95], v[180:183], v[0:15]\n"
    "ds_read_b64_tr_b16 v[84:85], v54 offset:49152\n"
    "ds_read_b64_tr_b16 v[86:87], v54 offset:49664\n"
    "v_add_f32 v58, v58, v63\n"
    "v_fmac_f32 v57, v62, v62\n"
    "v_fmac_f32 v59, v63, v63\n"
    "v_mfma_f32_4x4x4_16b_bf16 v[24:27], v[182:183], v[182:183], v[24:27]\n"
    "v_mfma_f32_4x4x4_16b_bf16 v[28:31], v[100:101], v[182:183], v[28:31]\n"
    "s_setprio 0\n"
    "s_waitcnt lgkmcnt(2)\n"
    "v_mfma_f32_32x32x16_bf16 v[32:47], v[80:83], v[184:187], v[32:47]\n"
    "s_bfe_u32 s46, s72, 0x30008\n"
    "s_add_u32 m0, s50, 0x24000\n"
    "ds_read_b64_tr_b16 v[88:89], v55 offset:50176\n"
    "ds_read_b64_tr_b16 v[90:91], v55 offset:50688\n"
    "v_xor_b32 v51, 0x10000, v51\n"
    "ds_read_b128 v[188:191], v51 offset:49152\n"
    "ds_read_b64_tr_b16 v[92:93], v55 offset:49152\n"
    "ds_read_b64_tr_b16 v[94:95], v55 offset:49664\n"
    "s_mul_i32 s46, s46, s51\n"
    "buffer_load_dwordx4 v21, s[12:15], s46 offen lds\n"
    "s_waitcnt lgkmcnt(5)\n"
    "v_mfma_f32_32x32x16_bf16 v[0:15], v[84:87], v[184:187], v[0:15]\n"
    "s_waitcnt vmcnt(6)\n"
    "s_barrier\n"
    "s_setprio 3\n"
    "s_waitcnt lgkmcnt(2)\n"
    "v_mfma_f32_32x32x16_bf16 v[32:47], v[88:91], v[188:191], v[32:47]\n"
    "s_bfe_u32 s48, s72, 0x30018\n"
    "s_add_u32 m0, s49, 0\n"
    "ds_read_b64_tr_b16 v[80:81], v52 offset:17408\n"
    "ds_read_b64_tr_b16 v[82:83], v52 offset:17920\n"
    "v_xor_b32 v48, 0x10000, v48\n"
    "ds_read_b128 v[192:195], v48 offset:16384\n"
    "s_lshl_b32 s45, s48, 7\n"
    "buffer_load_dwordx4 v18, s[4:7], s45 offen lds\n"
    "s_waitcnt lgkmcnt(3)\n"
    "v_mfma_f32_32x32x16_bf16 v[0:15], v[92:95], v[188:191], v[0:15]\n"
    "ds_read_b64_tr_b16 v[84:85], v52 offset:16384\n"
    "ds_read_b64_tr_b16 v[86:87], v52 offset:16896\n"
    "s_setprio 2\n"
    "s_waitcnt lgkmcnt(2)\n"
    "v_mfma_f32_32x32x16_bf16 v[32:47], v[80:83], v[192:195], v[32:47]\n"
    "s_mul_i32 s47, s48, s51\n"
    "buffer_load_dwordx4 v19, s[4:7], s45 offen offset:1024 lds\n"
    "ds_read_b64_tr_b16 v[88:89], v53 offset:17408\n"
    "ds_read_b64_tr_b16 v[90:91], v53 offset:17920\n"
    "v_lshlrev_b32 v60, 16, v192\n"
    "v_and_b32 v61, s71, v192\n"
    "v_add_f32 v56, v56, v60\n"
    "v_add_f32 v58, v58, v61\n"
    "v_fmac_f32 v57, v60, v60\n"
    "v_mfma_f32_16x16x32_bf16 v[104:107], v[116:119], v[80:83], v[104:107]\n"
    "v_xor_b32 v49, 0x10000, v49\n"
    "ds_read_b128 v[196:199], v49 offset:16384\n"
    "v_fmac_f32 v59, v61, v61\n"
    "v_lshlrev_b32 v62, 16, v193\n"
    "v_and_b32 v63, s71, v193\n"
    "s_waitcnt lgkmcnt(3)\n"
    "v_mfma_f32_32x32x16_bf16 v[0:15], v[84:87], v[192:195], v[0:15]\n"
    "s_add_u32 m0, s49, 0xc000\n"
    "s_mul_i32 s48, s48, 0x2a000\n"
    "ds_read_b64_tr_b16 v[92:93], v53 offset:16384\n"
    "buffer_load_dwordx4 v79, s[8:11], s47 offen lds\n"
    "v_add_f32 v56, v56, v62\n"
    "v_add_f32 v58, v58, v63\n"
    "v_fmac_f32 v57, v62, v62\n"
    "v_fmac_f32 v59, v63, v63\n"
    "v_lshlrev_b32 v60, 16, v194\n"
    "v_mfma_f32_16x16x32_bf16 v[108:111], v[116:119], v[84:87], v[108:111]\n"
    "ds_read_b64_tr_b16 v[94:95], v53 offset:16896\n"
    "v_and_b32 v61, s71, v194\n"
    "v_add_f32 v56, v56, v60\n"
    "v_add_f32 v58, v58, v61\n"
    "s_setprio 1\n"
    "s_waitcnt lgkmcnt(2)\n"
    "v_mfma_f32_32x32x16_bf16 v[32:47], v[88:91], v[196:199], v[32:47]\n"
    "buffer_load_dwordx4 v20, s[16:19], s48 offen offset:1024 lds\n"
    "ds_read_b64_tr_b16 v[80:81], v54 offset:17408\n"
    "ds_read_b64_tr_b16 v[82:83], v54 offset:17920\n"
    "v_xor_b32 v50, 0x10000, v50\n"
    "ds_read_b128 v[200:203], v50 offset:16384\n"
    "v_fmac_f32 v57, v60, v60\n"
    "v_fmac_f32 v59, v61, v61\n"
    "v_lshlrev_b32 v62, 16, v195\n"
    "v_and_b32 v63, s71, v195\n"
    "v_add_f32 v56, v56, v62\n"
    "v_mfma_f32_4x4x4_16b_bf16 v[24:27], v[196:197], v[196:197], v[24:27]\n"
    "v_mfma_f32_4x4x4_16b_bf16 v[28:31], v[100:101], v[196:197], v[28:31]\n"
    "s_waitcnt lgkmcnt(3)\n"
    "v_mfma_f32_32x32x16_bf16 v[0:15], v[92:95], v[196:199], v[0:15]\n"
    "ds_read_b64_tr_b16 v[84:85], v54 offset:16384\n"
    "ds_read_b64_tr_b16 v[86:87], v54 offset:16896\n"
    "v_add_f32 v58, v58, v63\n"
    "v_fmac_f32 v57, v62, v62\n"
    "v_fmac_f32 v59, v63, v63\n"
    "v_mfma_f32_4x4x4_16b_bf16 v[24:27], v[198:199], v[198:199], v[24:27]\n"
    "v_mfma_f32_4x4x4_16b_bf16 v[28:31], v[100:101], v[198:199], v[28:31]\n"
    "s_setprio 0\n"
    "s_waitcnt lgkmcnt(2)\n"
    "v_mfma_f32_32x32x16_bf16 v[32:47], v[80:83], v[200:203], v[32:47]\n"
    "ds_read_b64_tr_b16 v[88:89], v55 offset:17408\n"
    "ds_read_b64_tr_b16 v[90:91], v55 offset:17920\n"
    "v_xor_b32 v51, 0x10000, v51\n"
    "ds_read_b128 v[204:207], v51 offset:16384\n"
    "ds_read_b64_tr_b16 v[92:93], v55 offset:16384\n"
    "ds_read_b64_tr_b16 v[94:95], v55 offset:16896\n"
    "s_waitcnt lgkmcnt(5)\n"
    "v_mfma_f32_32x32x16_bf16 v[0:15], v[84:87], v[200:203], v[0:15]\n"
    "s_waitcnt vmcnt(5)\n"
    "s_barrier\n"
    "s_setprio 3\n"
    "s_waitcnt lgkmcnt(2)\n"
    "v_mfma_f32_32x32x16_bf16 v[32:47], v[88:91], v[204:207], v[32:47]\n"
    "s_bfe_u32 s46, s72, 0x3000c\n"
    "s_add_u32 m0, s50, 0x18000\n"
    "ds_read_b64_tr_b16 v[80:81], v52 offset:33792\n"
    "ds_read_b64_tr_b16 v[82:83], v52 offset:34304\n"
    "ds_read_b128 v[208:211], v48 offset:32768\n"
    "s_mul_i32 s46, s46, s51\n"
    "buffer_load_dwordx4 v21, s[12:15], s46 offen lds\n"
    "s_waitcnt lgkmcnt(3)\n"
    "v_mfma_f32_32x32x16_bf16 v[0:15], v[92:95], v[204:207], v[0:15]\n"
    "ds_read_b64_tr_b16 v[84:85], v52 offset:32768\n"
    "ds_read_b64_tr_b16 v[86:87], v52 offset:33280\n"
    "s_setprio 2\n"
    "s_waitcnt lgkmcnt(2)\n"
    "v_mfma_f32_32x32x16_bf16 v[32:47], v[80:83], v[208:211], v[32:47]\n"
    "s_bfe_u32 s46, s72, 0x30010\n"
    "s_add_u32 m0, s50, 0x1a000\n"
    "ds_read_b64_tr_b16 v[88:89], v53 offset:33792\n"
    "ds_read_b64_tr_b16 v[90:91], v53 offset:34304\n"
    "s_mul_i32 s46, s46, s51\n"
    "buffer_load_dwordx4 v21, s[12:15], s46 offen lds\n"
    "v_lshlrev_b32 v60, 16, v208\n"
    "v_and_b32 v61, s71, v208\n"
    "v_add_f32 v56, v56, v60\n"
    "v_add_f32 v58, v58, v61\n"
    "v_fmac_f32 v57, v60, v60\n"
    "v_mfma_f32_16x16x32_bf16 v[104:107], v[116:119], v[80:83], v[104:107]\n"
    "ds_read_b128 v[212:215], v49 offset:32768\n"
    "v_fmac_f32 v59, v61, v61\n"
    "v_lshlrev_b32 v62, 16, v209\n"
    "v_and_b32 v63, s71, v209\n"
    "s_waitcnt lgkmcnt(3)\n"
    "v_mfma_f32_32x32x16_bf16 v[0:15], v[84:87], v[208:211], v[0:15]\n"
    "s_bfe_u32 s46, s72, 0x30014\n"
    "s_add_u32 m0, s50, 0x1c000\n"
    "ds_read_b64_tr_b16 v[92:93], v53 offset:32768\n"
    "s_mul_i32 s46, s46, s51\n"
    "buffer_load_dwordx4 v21, s[12:15], s46 offen lds\n"
    "v_add_f32 v56, v56, v62\n"
    "v_add_f32 v58, v58, v63\n"
    "v_fmac_f32 v57, v62, v62\n"
    "v_fmac_f32 v59, v63, v63\n"
    "v_lshlrev_b32 v60, 16, v210\n"
    "v_mfma_f32_16x16x32_bf16 v[108:111], v[116:119], v[84:87], v[108:111]\n"
    "ds_read_b64_tr_b16 v[94:95], v53 offset:33280\n"
    "v_and_b32 v61, s71, v210\n"
    "v_add_f32 v56, v56, v60\n"
    "v_add_f32 v58, v58, v61\n"
    "s_setprio 1\n"
    "s_waitcnt lgkmcnt(2)\n"
    "v_mfma_f32_32x32x16_bf16 v[32:47], v[88:91], v[212:215], v[32:47]\n"
    "s_bfe_u32 s46, s72, 0x30018\n"
    "s_add_u32 m0, s50, 0x1e000\n"
    "ds_read_b64_tr_b16 v[80:81], v54 offset:33792\n"
    "ds_read_b64_tr_b16 v[82:83], v54 offset:34304\n"
    "ds_read_b128 v[216:219], v50 offset:32768\n"
    "s_mul_i32 s46, s46, s51\n"
    "buffer_load_dwordx4 v21, s[12:15], s46 offen lds\n"
    "v_fmac_f32 v57, v60, v60\n"
    "v_fmac_f32 v59, v61, v61\n"
    "v_lshlrev_b32 v62, 16, v211\n"
    "v_and_b32 v63, s71, v211\n"
    "v_add_f32 v56, v56, v62\n"
    "v_mfma_f32_4x4x4_16b_bf16 v[24:27], v[212:213], v[212:213], v[24:27]\n"
    "v_mfma_f32_4x4x4_16b_bf16 v[28:31], v[100:101], v[212:213], v[28:31]\n"
    "s_waitcnt lgkmcnt(3)\n"
    "v_mfma_f32_32x32x16_bf16 v[0:15], v[92:95], v[212:215], v[0:15]\n"
    "ds_read_b64_tr_b16 v[84:85], v54 offset:32768\n"
    "ds_read_b64_tr_b16 v[86:87], v54 offset:33280\n"
    "v_add_f32 v58, v58, v63\n"
    "v_fmac_f32 v57, v62, v62\n"
    "v_fmac_f32 v59, v63, v63\n"
    "s_mov_b32 vcc_lo, 0xaaaaaaaa\n"
    "s_mov_b32 vcc_hi, 0xaaaaaaaa\n"
    "s_mov_b32 s36, 0xcccccccc\n"
    "s_mov_b32 s37, 0xcccccccc\n"
    "v_mfma_f32_4x4x4_16b_bf16 v[24:27], v[214:215], v[214:215], v[24:27]\n"
    "v_mfma_f32_4x4x4_16b_bf16 v[28:31], v[100:101], v[214:215], v[28:31]\n"
    "s_setprio 0\n"
    "s_waitcnt lgkmcnt(2)\n"
    "v_mfma_f32_32x32x16_bf16 v[32:47], v[80:83], v[216:219], v[32:47]\n"
    "ds_read_b64_tr_b16 v[88:89], v55 offset:33792\n"
    "ds_read_b64_tr_b16 v[90:91], v55 offset:34304\n"
    "ds_read_b128 v[220:223], v51 offset:32768\n"
    "ds_read_b64_tr_b16 v[92:93], v55 offset:32768\n"
    "ds_read_b64_tr_b16 v[94:95], v55 offset:33280\n"
    "s_waitcnt lgkmcnt(5)\n"
    "v_mfma_f32_32x32x16_bf16 v[0:15], v[84:87], v[216:219], v[0:15]\n"
    "s_waitcnt vmcnt(4)\n"
    "s_barrier\n"
    "s_setprio 3\n"
    "s_waitcnt lgkmcnt(2)\n"
    "v_mfma_f32_32x32x16_bf16 v[32:47], v[88:91], v[220:223], v[32:47]\n"
    "s_mov_b64 s[8:9], s[30:31]\n"
    "s_mov_b32 s10, 0x188000\n"
    "ds_read_b64_tr_b16 v[80:81], v52 offset:1024\n"
    "ds_read_b64_tr_b16 v[82:83], v52 offset:1536\n"
    "ds_read_b128 v[224:227], v48\n"
    "s_mov_b32 s31, 0x20000\n"
    "s_bfe_u32 s46, s72, 0x30000\n"
    "s_add_u32 m0, s50, 0x4000\n"
    "s_mul_i32 s46, s46, 0x38000\n"
    "buffer_load_dwordx4 v78, s[8:11], s46 offen lds\n"
    "s_waitcnt lgkmcnt(3)\n"
    "v_mfma_f32_32x32x16_bf16 v[0:15], v[92:95], v[220:223], v[0:15]\n"
    "ds_read_b64_tr_b16 v[84:85], v52\n"
    "ds_read_b64_tr_b16 v[86:87], v52 offset:512\n"
    "s_setprio 2\n"
    "s_waitcnt lgkmcnt(2)\n"
    "v_mfma_f32_32x32x16_bf16 v[32:47], v[80:83], v[224:227], v[32:47]\n"
    "s_bfe_u32 s46, s72, 0x30004\n"
    "s_add_u32 m0, s50, 0x6000\n"
    "ds_read_b64_tr_b16 v[88:89], v53 offset:1024\n"
    "ds_read_b64_tr_b16 v[90:91], v53 offset:1536\n"
    "s_mul_i32 s46, s46, 0x38000\n"
    "buffer_load_dwordx4 v78, s[8:11], s46 offen lds\n"
    "v_lshlrev_b32 v60, 16, v224\n"
    "v_and_b32 v61, s71, v224\n"
    "v_add_f32 v56, v56, v60\n"
    "v_add_f32 v58, v58, v61\n"
    "v_fmac_f32 v57, v60, v60\n"
    "v_fmac_f32 v59, v61, v61\n"
    "v_lshlrev_b32 v62, 16, v225\n"
    "v_and_b32 v63, s71, v225\n"
    "v_mfma_f32_16x16x32_bf16 v[104:107], v[116:119], v[80:83], v[104:107]\n"
    "ds_read_b128 v[228:231], v49\n"
    "v_add_f32 v56, v56, v62\n"
    "v_add_f32 v58, v58, v63\n"
    "v_fmac_f32 v57, v62, v62\n"
    "v_fmac_f32 v59, v63, v63\n"
    "v_lshlrev_b32 v60, 16, v226\n"
    "v_and_b32 v61, s71, v226\n"
    "s_waitcnt lgkmcnt(3)\n"
    "v_mfma_f32_32x32x16_bf16 v[0:15], v[84:87], v[224:227], v[0:15]\n"
    "s_bfe_u32 s46, s72, 0x30008\n"
    "s_add_u32 m0, s50, 0x10000\n"
    "ds_read_b64_tr_b16 v[92:93], v53\n"
    "s_mul_i32 s46, s46, 0x38000\n"
    "buffer_load_dwordx4 v78, s[8:11], s46 offen lds\n"
    "v_add_f32 v56, v56, v60\n"
    "v_add_f32 v58, v58, v61\n"
    "v_fmac_f32 v57, v60, v60\n"
    "v_fmac_f32 v59, v61, v61\n"
    "v_lshlrev_b32 v62, 16, v227\n"
    "v_and_b32 v63, s71, v227\n"
    "v_add_f32 v56, v56, v62\n"
    "v_add_f32 v58, v58, v63\n"
    "v_mfma_f32_16x16x32_bf16 v[108:111], v[116:119], v[84:87], v[108:111]\n"
    "ds_read_b64_tr_b16 v[94:95], v53 offset:512\n"
    "v_fmac_f32 v57, v62, v62\n"
    "v_fmac_f32 v59, v63, v63\n"
    "s_setprio 1\n"
    "s_waitcnt lgkmcnt(2)\n"
    "v_mfma_f32_32x32x16_bf16 v[32:47], v[88:91], v[228:231], v[32:47]\n"
    "s_bfe_u32 s46, s72, 0x3000c\n"
    "s_add_u32 m0, s50, 0x12000\n"
    "ds_read_b64_tr_b16 v[80:81], v54 offset:1024\n"
    "ds_read_b64_tr_b16 v[82:83], v54 offset:1536\n"
    "ds_read_b128 v[232:235], v50\n"
    "s_mul_i32 s46, s46, 0x38000\n"
    "buffer_load_dwordx4 v78, s[8:11], s46 offen lds\n"
    "v_and_b32 v105, s71, v104\n"
    "v_sub_f32 v104, v104, v105\n"
    "v_lshrrev_b32 v104, 16, v104\n"
    "v_or_b32 v105, v105, v104\n"
    "ds_write_b32 v247, v105\n"
    "v_and_b32 v109, s71, v108\n"
    "v_sub_f32 v108, v108, v109\n"
    "v_lshrrev_b32 v108, 16, v108\n"
    "v_or_b32 v109, v109, v108\n"
    "v_mfma_f32_4x4x4_16b_bf16 v[24:27], v[228:229], v[228:229], v[24:27]\n"
    "v_mfma_f32_4x4x4_16b_bf16 v[28:31], v[100:101], v[228:229], v[28:31]\n"
    "s_waitcnt lgkmcnt(4)\n"
    "v_mfma_f32_32x32x16_bf16 v[0:15], v[92:95], v[228:231], v[0:15]\n"
    "ds_read_b64_tr_b16 v[84:85], v54\n"
    "ds_read_b64_tr_b16 v[86:87], v54 offset:512\n"
    "ds_write_b32 v247, v109 offset:1024\n"
    "v_mfma_f32_4x4x4_16b_bf16 v[24:27], v[230:231], v[230:231], v[24:27]\n"
    "v_mfma_f32_4x4x4_16b_bf16 v[28:31], v[100:101], v[230:231], v[28:31]\n"
    "s_setprio 0\n"
    "s_waitcnt lgkmcnt(4)\n"
    "v_mfma_f32_32x32x16_bf16 v[32:47], v[80:83], v[232:235], v[32:47]\n"
    "ds_read_b64_tr_b16 v[88:89], v55 offset:1024\n"
    "ds_read_b64_tr_b16 v[90:91], v55 offset:1536\n"
    "ds_read_b128 v[236:239], v51\n"
    "ds_read_b64_tr_b16 v[92:93], v55\n"
    "ds_read_b64_tr_b16 v[94:95], v55 offset:512\n"
    "v_add_f32 v56, v56, v58\n"
    "v_add_f32 v57, v57, v59\n"
    "v_cndmask_b32 v24, v24, v25, vcc\n"
    "v_cndmask_b32 v26, v26, v27, vcc\n"
    "v_add_f32 v56, v56, v28\n"
    "v_cndmask_b32_e64 v24, v24, v26, s[36:37]\n"
    "v_add_f32 v57, v57, v24\n"
    "ds_write_b64 v16, v[56:57]\n"
    "s_waitcnt lgkmcnt(7)\n"
    "v_mfma_f32_32x32x16_bf16 v[0:15], v[84:87], v[232:235], v[0:15]\n"
    "s_waitcnt vmcnt(7) lgkmcnt(0)\n"
    "s_barrier\n"
    "s_setprio 3\n"
    "ds_read_b128 v[108:111], v17\n"
    "ds_read_b128 v[112:115], v17 offset:16\n"
    "ds_read_b128 v[24:27], v248\n"
    "v_mfma_f32_32x32x16_bf16 v[32:47], v[88:91], v[236:239], v[32:47]\n"
    "ds_read_b64_tr_b16 v[80:81], v124 offset:32768\n"
    "ds_read_b64_tr_b16 v[82:83], v124 offset:33280\n"
    "ds_read_b64_tr_b16 v[84:85], v125 offset:32768\n"
    "v_mfma_f32_32x32x16_bf16 v[0:15], v[92:95], v[236:239], v[0:15]\n"
    "ds_read_b64_tr_b16 v[86:87], v125 offset:33280\n"
    "ds_read_b128 v[28:31], v248 offset:1024\n"
    "ds_read_b64_tr_b16 v[88:89], v126 offset:32768\n"
    "ds_read_b64_tr_b16 v[90:91], v126 offset:33280\n"
    "s_cmp_eq_u32 s52, 0\n"
    "s_cbranch_scc0 .Lk_if22\n"
    "s_setprio 3\n"
    "s_waitcnt lgkmcnt(5)\n"
    "v_mfma_f32_32x32x16_bf16 v[48:63], v[80:83], v[128:131], 0\n"
    "ds_read_b64_tr_b16 v[92:93], v127 offset:32768\n"
    "ds_read_b64_tr_b16 v[94:95], v127 offset:33280\n"
    "s_bfe_u32 s46, s72, 0x30010\n"
    "s_add_u32 m0, s50, 0x8000\n"
    "s_mul_i32 s46, s46, 0x38000\n"
    "buffer_load_dwordx4 v78, s[8:11], s46 offen lds\n"
    "v_add_f32 v108, v108, v110\n"
    "v_add_f32 v112, v112, v114\n"
    "v_add_f32 v109, v109, v111\n"
    "v_add_f32 v113, v113, v115\n"
    "v_add_f32 v108, v108, v112\n"
    "v_add_f32 v109, v109, v113\n"
    "v_mul_f32 v108, 0x3b124925, v108\n"
    "v_cmp_gt_u32 vcc, 0x200, v244\n"
    "v_mul_f32 v109, 0x3b124925, v109\n"
    "v_cvt_pk_bf16_f32 v110, -v108, -v108\n"
    "v_fma_f32 v109, -v108, v108, v109\n"
    "v_cndmask_b32 v120, 0, v110, vcc\n"
    "v_mfma_f32_16x16x32_bf16 v[104:107], v[116:119], v[80:83], 0\n"
    "v_mov_b32 v121, v120\n"
    "v_mov_b32 v122, v120\n"
    "s_waitcnt lgkmcnt(5)\n"
    "v_mfma_f32_32x32x16_bf16 v[48:63], v[84:87], v[132:135], v[48:63]\n"
    "ds_read_b64_tr_b16 v[80:81], v124 offset:40960\n"
    "ds_read_b64_tr_b16 v[82:83], v124 offset:41472\n"
    "v_mov_b32 v123, v120\n"
    "s_nop 1\n"
    "v_mfma_f32_32x32x16_bf16 v[32:47], v[24:27], v[120:123], v[32:47]\n"
    "v_max_f32 v109, 0, v109\n"
    "v_cvt_pk_bf16_f32 v109, 0, v109\n"
    "v_add_f32 v109, 0x3727c5ac, v109\n"
    "v_cvt_pk_bf16_f32 v109, 0, v109\n"
    "v_sqrt_f32 v109, v109\n"
    "v_add_u32 v240, 1, v240\n"
    "v_cvt_pk_bf16_f32 v109, 0, v109\n"
    "v_rcp_f32 v99, v109\n"
    "s_waitcnt lgkmcnt(4)\n"
    "v_mfma_f32_32x32x16_bf16 v[48:63], v[88:91], v[136:139], v[48:63]\n"
    "ds_read_b64_tr_b16 v[84:85], v125 offset:40960\n"
    "ds_read_b64_tr_b16 v[86:87], v125 offset:41472\n"
    "v_mfma_f32_32x32x16_bf16 v[0:15], v[28:31], v[120:123], v[0:15]\n"
    "v_mul_f32 v32, v99, v32\n"
    "v_mul_f32 v33, v99, v33\n"
    "v_cvt_pk_bf16_f32 v32, v32, v33\n"
    "v_mul_f32 v34, v99, v34\n"
    "v_mul_f32 v35, v99, v35\n"
    "v_cvt_pk_bf16_f32 v33, v34, v35\n"
    "v_mul_f32 v36, v99, v36\n"
    "v_mul_f32 v37, v99, v37\n"
    "v_cvt_pk_bf16_f32 v34, v36, v37\n"
    "s_waitcnt lgkmcnt(4)\n"
    "v_mfma_f32_32x32x16_bf16 v[48:63], v[92:95], v[140:143], v[48:63]\n"
    "ds_read_b64_tr_b16 v[88:89], v126 offset:40960\n"
    "ds_read_b64_tr_b16 v[90:91], v126 offset:41472\n"
    "v_mul_f32 v38, v99, v38\n"
    "v_mul_f32 v39, v99, v39\n"
    "v_cvt_pk_bf16_f32 v35, v38, v39\n"
    "v_mul_f32 v40, v99, v40\n"
    "v_mul_f32 v41, v99, v41\n"
    "v_cvt_pk_bf16_f32 v36, v40, v41\n"
    "v_mul_f32 v42, v99, v42\n"
    "v_mul_f32 v43, v99, v43\n"
    "v_cvt_pk_bf16_f32 v37, v42, v43\n"
    "v_mul_f32 v44, v99, v44\n"
    "v_mul_f32 v45, v99, v45\n"
    "v_cvt_pk_bf16_f32 v38, v44, v45\n"
    "s_setprio 2\n"
    "s_waitcnt lgkmcnt(4)\n"
    "v_mfma_f32_32x32x16_bf16 v[48:63], v[80:83], v[144:147], v[48:63]\n"
    "ds_read_b64_tr_b16 v[92:93], v127 offset:40960\n"
    "ds_read_b64_tr_b16 v[94:95], v127 offset:41472\n"
    "v_mul_f32 v46, v99, v46\n"
    "v_mul_f32 v47, v99, v47\n"
    "v_cvt_pk_bf16_f32 v39, v46, v47\n"
    "s_cmp_ge_u32 s53, 14\n"
    "s_cbranch_scc0 .Lk_if7\n"
    "ds_write_b64 v243, v[32:33]\n"
    "ds_write_b64 v243, v[34:35] offset:16\n"
    "ds_write_b64 v243, v[36:37] offset:32\n"
    "ds_write_b64 v243, v[38:39] offset:48\n"
    "s_branch .Lk_if7_end\n"
    ".Lk_if7:\n"
    "v_permlane32_swap_b32 v32, v34\n"
    "v_permlane32_swap_b32 v33, v35\n"
    "v_permlane32_swap_b32 v36, v38\n"
    "v_permlane32_swap_b32 v37, v39\n"
    "s_add_u32 s47, s56, 0x10000\n"
    "buffer_store_dwordx4 v[32:35], v245, s[20:23], s56 offen sc1\n"
    "buffer_store_dwordx4 v[36:39], v245, s[20:23], s47 offen sc1\n"
    ".Lk_if7_end:\n"
    "v_mul_f32 v98, v109, v109\n"
    "v_mul_f32 v96, 0xc0135761, v99\n"
    "v_mul_f32 v108, v99, v99\n"
    "v_mul_f32 v97, 0x3d372713, v96\n"
    "v_mul_f32 v97, v97, v108\n"
    "v_mul_f32 v100, v0, v0\n"
    "v_mul_f32 v101, v1, v1\n"
    "v_mul_f32 v102, v2, v2\n"
    "v_mul_f32 v103, v3, v3\n"
    "v_mfma_f32_16x16x32_bf16 v[104:107], v[116:119], v[80:83], v[104:107]\n"
    "s_cmp_ge_u32 s53, 14\n"
    "s_cbranch_scc0 .Lk_if8\n"
    "s_waitcnt lgkmcnt(0)\n"
    "ds_read_b64_tr_b16 v[40:41], v246\n"
    "ds_read_b64_tr_b16 v[42:43], v246 offset:512\n"
    "ds_read_b64_tr_b16 v[44:45], v246 offset:1024\n"
    "ds_read_b64_tr_b16 v[46:47], v246 offset:1536\n"
    ".Lk_if8:\n"
    "v_fma_f32 v100, v100, v97, v96\n"
    "s_waitcnt lgkmcnt(4)\n"
    "v_mfma_f32_32x32x16_bf16 v[48:63], v[84:87], v[148:151], v[48:63]\n"
    "ds_read_b64_tr_b16 v[80:81], v124 offset:49152\n"
    "ds_read_b64_tr_b16 v[82:83], v124 offset:49664\n"
    "v_fma_f32 v101, v101, v97, v96\n"
    "v_fma_f32 v102, v102, v97, v96\n"
    "v_fma_f32 v103, v103, v97, v96\n"
    "v_mul_f32 v100, v0, v100\n"
    "v_mul_f32 v101, v1, v101\n"
    "v_mul_f32 v102, v2, v102\n"
    "v_mul_f32 v103, v3, v103\n"
    "s_cmp_ge_u32 s53, 14\n"
    "s_cbranch_scc0 .Lk_if9\n"
    "s_waitcnt lgkmcnt(4)\n"
    "buffer_store_dwordx4 v[40:43], v242, s[20:23], s56 offen sc1\n"
    "s_waitcnt lgkmcnt(2)\n"
    "buffer_store_dwordx4 v[44:47], v242, s[20:23], s56 offen offset:2048 sc1\n"
    ".Lk_if9:\n"
    "v_exp_f32 v100, v100\n"
    "v_exp_f32 v101, v101\n"
    "s_waitcnt lgkmcnt(4)\n"
    "v_mfma_f32_32x32x16_bf16 v[48:63], v[88:91], v[152:155], v[48:63]\n"
    "ds_read_b64_tr_b16 v[84:85], v125 offset:49152\n"
    "ds_read_b64_tr_b16 v[86:87], v125 offset:49664\n"
    "v_exp_f32 v102, v102\n"
    "v_exp_f32 v103, v103\n"
    "s_waitcnt lgkmcnt(4)\n"
    "v_mfma_f32_32x32x16_bf16 v[48:63], v[92:95], v[156:159], v[48:63]\n"
    "ds_read_b64_tr_b16 v[88:89], v126 offset:49152\n"
    "ds_read_b64_tr_b16 v[90:91], v126 offset:49664\n"
    "v_fma_f32 v100, v100, v98, v98\n"
    "v_fma_f32 v101, v101, v98, v98\n"
    "v_fma_f32 v102, v102, v98, v98\n"
    "v_fma_f32 v103, v103, v98, v98\n"
    "s_setprio 1\n"
    "s_waitcnt lgkmcnt(4)\n"
    "v_mfma_f32_32x32x16_bf16 v[48:63], v[80:83], v[160:163], v[48:63]\n"
    "ds_read_b64_tr_b16 v[92:93], v127 offset:49152\n"
    "ds_read_b64_tr_b16 v[94:95], v127 offset:49664\n"
    "v_rcp_f32 v100, v100\n"
    "v_rcp_f32 v101, v101\n"
    "v_mfma_f32_16x16x32_bf16 v[104:107], v[116:119], v[80:83], v[104:107]\n"
    "v_rcp_f32 v102, v102\n"
    "s_waitcnt lgkmcnt(4)\n"
    "v_mfma_f32_32x32x16_bf16 v[48:63], v[84:87], v[164:167], v[48:63]\n"
    "ds_read_b64_tr_b16 v[80:81], v124\n"
    "ds_read_b64_tr_b16 v[82:83], v124 offset:512\n"
    "v_rcp_f32 v103, v103\n"
    "v_mul_f32 v0, v0, v100\n"
    "v_mul_f32 v1, v1, v101\n"
    "s_waitcnt lgkmcnt(4)\n"
    "v_mfma_f32_32x32x16_bf16 v[48:63], v[88:91], v[168:171], v[48:63]\n"
    "ds_read_b64_tr_b16 v[84:85], v125\n"
    "ds_read_b64_tr_b16 v[86:87], v125 offset:512\n"
    "v_mul_f32 v2, v2, v102\n"
    "v_mul_f32 v3, v3, v103\n"
    "v_mul_f32 v100, v4, v4\n"
    "v_mul_f32 v101, v5, v5\n"
    "s_waitcnt lgkmcnt(4)\n"
    "v_mfma_f32_32x32x16_bf16 v[48:63], v[92:95], v[172:175], v[48:63]\n"
    "ds_read_b64_tr_b16 v[88:89], v126\n"
    "ds_read_b64_tr_b16 v[90:91], v126 offset:512\n"
    "v_mul_f32 v102, v6, v6\n"
    "v_mul_f32 v103, v7, v7\n"
    "v_fma_f32 v100, v100, v97, v96\n"
    "v_fma_f32 v101, v101, v97, v96\n"
    "s_waitcnt vmcnt(5)\n"
    "s_barrier\n"
    "s_setprio 0\n"
    "s_waitcnt lgkmcnt(4)\n"
    "v_mfma_f32_32x32x16_bf16 v[48:63], v[80:83], v[176:179], v[48:63]\n"
    "ds_read_b64_tr_b16 v[92:93], v127\n"
    "ds_read_b64_tr_b16 v[94:95], v127 offset:512\n"
    "v_fma_f32 v102, v102, v97, v96\n"
    "v_fma_f32 v103, v103, v97, v96\n"
    "v_mul_f32 v100, v4, v100\n"
    "v_mul_f32 v101, v5, v101\n"
    "v_mfma_f32_16x16x32_bf16 v[104:107], v[116:119], v[80:83], v[104:107]\n"
    "v_mul_f32 v102, v6, v102\n"
    "v_mul_f32 v103, v7, v103\n"
    "s_waitcnt lgkmcnt(4)\n"
    "v_mfma_f32_32x32x16_bf16 v[48:63], v[84:87], v[180:183], v[48:63]\n"
    "ds_read_b64_tr_b16 v[80:81], v124 offset:8192\n"
    "ds_read_b64_tr_b16 v[82:83], v124 offset:8704\n"
    "s_bfe_u32 s46, s72, 0x30014\n"
    "s_add_u32 m0, s50, 0xa000\n"
    "s_mul_i32 s46, s46, 0x38000\n"
    "buffer_load_dwordx4 v78, s[8:11], s46 offen lds\n"
    "v_exp_f32 v100, v100\n"
    "v_exp_f32 v101, v101\n"
    "s_waitcnt lgkmcnt(4)\n"
    "v_mfma_f32_32x32x16_bf16 v[48:63], v[88:91], v[184:187], v[48:63]\n"
    "ds_read_b64_tr_b16 v[84:85], v125 offset:8192\n"
    "ds_read_b64_tr_b16 v[86:87], v125 offset:8704\n"
    "v_exp_f32 v102, v102\n"
    "v_exp_f32 v103, v103\n"
    "s_waitcnt lgkmcnt(4)\n"
    "v_mfma_f32_32x32x16_bf16 v[48:63], v[92:95], v[188:191], v[48:63]\n"
    "ds_read_b64_tr_b16 v[88:89], v126 offset:8192\n"
    "ds_read_b64_tr_b16 v[90:91], v126 offset:8704\n"
    "s_bfe_u32 s46, s72, 0x30018\n"
    "s_add_u32 m0, s50, 0xc000\n"
    "s_mul_i32 s46, s46, 0x38000\n"
    "buffer_load_dwordx4 v78, s[8:11], s46 offen lds\n"
    "v_fma_f32 v100, v100, v98, v98\n"
    "v_fma_f32 v101, v101, v98, v98\n"
    "v_fma_f32 v102, v102, v98, v98\n"
    "v_fma_f32 v103, v103, v98, v98\n"
    "s_setprio 3\n"
    "s_waitcnt lgkmcnt(4)\n"
    "v_mfma_f32_32x32x16_bf16 v[48:63], v[80:83], v[192:195], v[48:63]\n"
    "ds_read_b64_tr_b16 v[92:93], v127 offset:8192\n"
    "ds_read_b64_tr_b16 v[94:95], v127 offset:8704\n"
    "v_rcp_f32 v100, v100\n"
    "v_rcp_f32 v101, v101\n"
    "v_mfma_f32_16x16x32_bf16 v[104:107], v[116:119], v[80:83], v[104:107]\n"
    "v_rcp_f32 v102, v102\n"
    "s_waitcnt lgkmcnt(4)\n"
    "v_mfma_f32_32x32x16_bf16 v[48:63], v[84:87], v[196:199], v[48:63]\n"
    "ds_read_b64_tr_b16 v[80:81], v124 offset:16384\n"
    "ds_read_b64_tr_b16 v[82:83], v124 offset:16896\n"
    "s_xor_b32 s60, s73, 0\n"
    "s_mul_i32 s60, s60, 0xe000\n"
    "s_add_u32 m0, s50, 0xe000\n"
    "s_add_u32 s46, s60, 0\n"
    "buffer_load_dwordx4 v249, s[24:27], s46 offen lds\n"
    "v_rcp_f32 v103, v103\n"
    "v_mul_f32 v4, v4, v100\n"
    "v_mul_f32 v5, v5, v101\n"
    "s_waitcnt lgkmcnt(4)\n"
    "v_mfma_f32_32x32x16_bf16 v[48:63], v[88:91], v[200:203], v[48:63]\n"
    "ds_read_b64_tr_b16 v[84:85], v124 offset:24576\n"
    "ds_read_b64_tr_b16 v[86:87], v124 offset:25088\n"
    "v_mul_f32 v6, v6, v102\n"
    "v_mul_f32 v7, v7, v103\n"
    "v_mul_f32 v100, v8, v8\n"
    "v_mul_f32 v101, v9, v9\n"
    "s_waitcnt lgkmcnt(4)\n"
    "v_mfma_f32_32x32x16_bf16 v[48:63], v[92:95], v[204:207], v[48:63]\n"
    "ds_read_b64_tr_b16 v[88:89], v125 offset:16384\n"
    "ds_read_b64_tr_b16 v[90:91], v125 offset:16896\n"
    "s_mov_b64 s[12:13], s[34:35]\n"
    "s_mov_b32 s14, 0x188000\n"
    "s_mov_b32 s34, 0x1c0000\n"
    "s_mov_b32 s35, 0x20000\n"
    "s_bfe_u32 s46, s72, 0x30000\n"
    "s_add_u32 m0, s50, 0x20000\n"
    "s_mul_i32 s46, s46, 0x38000\n"
    "buffer_load_dwordx4 v78, s[12:15], s46 offen lds\n"
    "v_mul_f32 v102, v10, v10\n"
    "v_mul_f32 v103, v11, v11\n"
    "v_fma_f32 v100, v100, v97, v96\n"
    "v_fma_f32 v101, v101, v97, v96\n"
    "s_setprio 2\n"
    "s_waitcnt lgkmcnt(4)\n"
    "v_mfma_f32_32x32x16_bf16 v[48:63], v[80:83], v[208:211], v[48:63]\n"
    "ds_read_b64_tr_b16 v[92:93], v126 offset:16384\n"
    "ds_read_b64_tr_b16 v[94:95], v126 offset:16896\n"
    "v_fma_f32 v102, v102, v97, v96\n"
    "v_fma_f32 v103, v103, v97, v96\n"
    "v_mul_f32 v100, v8, v100\n"
    "v_mul_f32 v101, v9, v101\n"
    "v_mfma_f32_16x16x32_bf16 v[104:107], v[116:119], v[80:83], v[104:107]\n"
    "v_mul_f32 v102, v10, v102\n"
    "v_mul_f32 v103, v11, v103\n"
    "s_waitcnt lgkmcnt(4)\n"
    "v_mfma_f32_32x32x16_bf16 v[48:63], v[84:87], v[224:227], v[48:63]\n"
    "ds_read_b64_tr_b16 v[80:81], v127 offset:16384\n"
    "ds_read_b64_tr_b16 v[82:83], v127 offset:16896\n"
    "s_add_u32 m0, s50, 0x14000\n"
    "s_add_u32 s46, s60, 0x38000\n"
    "buffer_load_dwordx4 v249, s[24:27], s46 offen lds\n"
    "v_exp_f32 v100, v100\n"
    "v_exp_f32 v101, v101\n"
    "v_mfma_f32_16x16x32_bf16 v[104:107], v[116:119], v[84:87], v[104:107]\n"
    "v_exp_f32 v102, v102\n"
    "s_waitcnt lgkmcnt(4)\n"
    "v_mfma_f32_32x32x16_bf16 v[48:63], v[88:91], v[212:215], v[48:63]\n"
    "ds_read_b64_tr_b16 v[84:85], v125 offset:24576\n"
    "ds_read_b64_tr_b16 v[86:87], v125 offset:25088\n"
    "v_exp_f32 v103, v103\n"
    "v_fma_f32 v100, v100, v98, v98\n"
    "v_fma_f32 v101, v101, v98, v98\n"
    "s_waitcnt lgkmcnt(4)\n"
    "v_mfma_f32_32x32x16_bf16 v[48:63], v[92:95], v[216:219], v[48:63]\n"
    "ds_read_b64_tr_b16 v[88:89], v126 offset:24576\n"
    "ds_read_b64_tr_b16 v[90:91], v126 offset:25088\n"
    "v_and_b32 v105, s71, v104\n"
    "v_sub_f32 v104, v104, v105\n"
    "v_lshrrev_b32 v104, 16, v104\n"
    "v_or_b32 v105, v105, v104\n"
    "ds_write_b32 v247, v105 offset:2048\n"
    "s_add_u32 m0, s50, 0x16000\n"
    "s_add_u32 s46, s60, 0x70000\n"
    "buffer_load_dwordx4 v249, s[24:27], s46 offen lds\n"
    "s_setprio 1\n"
    "s_waitcnt lgkmcnt(5)\n"
    "v_mfma_f32_32x32x16_bf16 v[48:63], v[80:83], v[220:223], v[48:63]\n"
    "ds_read_b64_tr_b16 v[92:93], v127 offset:24576\n"
    "ds_read_b64_tr_b16 v[94:95], v127 offset:25088\n"
    "v_fma_f32 v102, v102, v98, v98\n"
    "v_fma_f32 v103, v103, v98, v98\n"
    "v_rcp_f32 v100, v100\n"
    "s_waitcnt vmcnt(6)\n"
    "s_waitcnt lgkmcnt(0)\n"
    "s_barrier\n"
    "v_mfma_f32_32x32x16_bf16 v[48:63], v[84:87], v[228:231], v[48:63]\n"
    "s_cmp_eq_u32 s40, 0\n"
    "s_cbranch_scc0 .Lk_if10\n"
    "s_mov_b64 exec, 0xff\n"
    "buffer_store_dwordx4 v[240:243], v244, s[20:23], s58 offen sc1\n"
    "s_mov_b64 exec, -1\n"
    ".Lk_if10:\n"
    "ds_read_b128 v[108:111], v248 offset:2048\n"
    "v_add_u32 v124, 0xfffec000, v124\n"
    "ds_read_b64_tr_b16 v[80:81], v124\n"
    "ds_read_b64_tr_b16 v[82:83], v124 offset:512\n"
    "v_rcp_f32 v101, v101\n"
    "v_rcp_f32 v102, v102\n"
    "v_mfma_f32_32x32x16_bf16 v[48:63], v[88:91], v[232:235], v[48:63]\n"
    "v_add_u32 v125, 0xfffec000, v125\n"
    "ds_read_b64_tr_b16 v[84:85], v125\n"
    "ds_read_b64_tr_b16 v[86:87], v125 offset:512\n"
    "v_rcp_f32 v103, v103\n"
    "v_mul_f32 v8, v8, v100\n"
    "v_mul_f32 v9, v9, v101\n"
    "v_mfma_f32_32x32x16_bf16 v[48:63], v[92:95], v[236:239], v[48:63]\n"
    "v_add_u32 v126, 0xfffec000, v126\n"
    "ds_read_b64_tr_b16 v[88:89], v126\n"
    "ds_read_b64_tr_b16 v[90:91], v126 offset:512\n"
    "s_bfe_u32 s46, s72, 0x30004\n"
    "s_add_u32 m0, s50, 0x22000\n"
    "s_mul_i32 s46, s46, 0x38000\n"
    "buffer_load_dwordx4 v78, s[12:15], s46 offen lds\n"
    "v_mul_f32 v10, v10, v102\n"
    "v_mul_f32 v11, v11, v103\n"
    "v_mul_f32 v100, v12, v12\n"
    "v_mul_f32 v101, v13, v13\n"
    "s_waitcnt lgkmcnt(6)\n"
    "v_mfma_f32_32x32x16_bf16 v[48:63], v[108:111], v[120:123], v[48:63]\n"
    "v_mul_f32 v102, v14, v14\n"
    "v_mul_f32 v103, v15, v15\n"
    "v_fma_f32 v100, v100, v97, v96\n"
    "v_fma_f32 v101, v101, v97, v96\n"
    "s_setprio 0\n"
    "s_waitcnt lgkmcnt(4)\n"
    "v_mfma_f32_32x32x16_bf16 v[16:31], v[80:83], v[128:131], 0\n"
    "v_add_u32 v127, 0xfffec000, v127\n"
    "ds_read_b64_tr_b16 v[92:93], v127\n"
    "ds_read_b64_tr_b16 v[94:95], v127 offset:512\n"
    "v_fma_f32 v102, v102, v97, v96\n"
    "v_fma_f32 v103, v103, v97, v96\n"
    "v_mul_f32 v100, v12, v100\n"
    "v_mul_f32 v101, v13, v101\n"
    "v_mfma_f32_16x16x32_bf16 v[104:107], v[116:119], v[80:83], 0\n"
    "v_mul_f32 v102, v14, v102\n"
    "v_mul_f32 v103, v15, v103\n"
    "s_waitcnt lgkmcnt(4)\n"
    "v_mfma_f32_32x32x16_bf16 v[16:31], v[84:87], v[132:135], v[16:31]\n"
    "ds_read_b64_tr_b16 v[80:81], v124 offset:8192\n"
    "ds_read_b64_tr_b16 v[82:83], v124 offset:8704\n"
    "s_bfe_u32 s46, s72, 0x30008\n"
    "s_add_u32 m0, s50, 0x24000\n"
    "s_mul_i32 s46, s46, 0x38000\n"
    "buffer_load_dwordx4 v78, s[12:15], s46 offen lds\n"
    "v_exp_f32 v100, v100\n"
    "v_exp_f32 v101, v101\n"
    "s_waitcnt lgkmcnt(4)\n"
    "v_mfma_f32_32x32x16_bf16 v[16:31], v[88:91], v[136:139], v[16:31]\n"
    "ds_read_b64_tr_b16 v[84:85], v125 offset:8192\n"
    "ds_read_b64_tr_b16 v[86:87], v125 offset:8704\n"
    "v_exp_f32 v102, v102\n"
    "v_exp_f32 v103, v103\n"
    "s_waitcnt lgkmcnt(4)\n"
    "v_mfma_f32_32x32x16_bf16 v[16:31], v[92:95], v[140:143], v[16:31]\n"
    "ds_read_b64_tr_b16 v[88:89], v126 offset:8192\n"
    "ds_read_b64_tr_b16 v[90:91], v126 offset:8704\n"
    "s_bfe_u32 s46, s72, 0x3000c\n"
    "s_add_u32 m0, s50, 0x18000\n"
    "s_mul_i32 s46, s46, 0x38000\n"
    "buffer_load_dwordx4 v78, s[12:15], s46 offen lds\n"
    "v_fma_f32 v100, v100, v98, v98\n"
    "v_fma_f32 v101, v101, v98, v98\n"
    "v_fma_f32 v102, v102, v98, v98\n"
    "v_fma_f32 v103, v103, v98, v98\n"
    "s_setprio 3\n"
    "s_waitcnt lgkmcnt(4)\n"
    "v_mfma_f32_32x32x16_bf16 v[16:31], v[80:83], v[144:147], v[16:31]\n"
    "ds_read_b64_tr_b16 v[92:93], v127 offset:8192\n"
    "ds_read_b64_tr_b16 v[94:95], v127 offset:8704\n"
    "v_rcp_f32 v100, v100\n"
    "v_rcp_f32 v101, v101\n"
    "v_mfma_f32_16x16x32_bf16 v[104:107], v[116:119], v[80:83], v[104:107]\n"
    "v_rcp_f32 v102, v102\n"
    "s_waitcnt lgkmcnt(4)\n"
    "v_mfma_f32_32x32x16_bf16 v[16:31], v[84:87], v[148:151], v[16:31]\n"
    "ds_read_b64_tr_b16 v[80:81], v124 offset:49152\n"
    "ds_read_b64_tr_b16 v[82:83], v124 offset:49664\n"
    "s_bfe_u32 s46, s72, 0x30010\n"
    "s_add_u32 m0, s50, 0x1a000\n"
    "s_mul_i32 s46, s46, 0x38000\n"
    "buffer_load_dwordx4 v78, s[12:15], s46 offen lds\n"
    "v_rcp_f32 v103, v103\n"
    "v_mul_f32 v12, v12, v100\n"
    "v_mul_f32 v13, v13, v101\n"
    "s_waitcnt lgkmcnt(4)\n"
    "v_mfma_f32_32x32x16_bf16 v[16:31], v[88:91], v[152:155], v[16:31]\n"
    "ds_read_b64_tr_b16 v[84:85], v125 offset:49152\n"
    "ds_read_b64_tr_b16 v[86:87], v125 offset:49664\n"
    "v_mul_f32 v14, v14, v102\n"
    "v_mul_f32 v15, v15, v103\n"
    "s_sub_u32 s56, s57, 0xc000\n"
    "v_mul_f32 v48, v0, v48\n"
    "s_waitcnt lgkmcnt(4)\n"
    "v_mfma_f32_32x32x16_bf16 v[16:31], v[92:95], v[156:159], v[16:31]\n"
    "ds_read_b64_tr_b16 v[88:89], v126 offset:49152\n"
    "ds_read_b64_tr_b16 v[90:91], v126 offset:49664\n"
    "s_bfe_u32 s46, s72, 0x30014\n"
    "s_add_u32 m0, s50, 0x1c000\n"
    "s_mul_i32 s46, s46, 0x38000\n"
    "buffer_load_dwordx4 v78, s[12:15], s46 offen lds\n"
    "v_mul_f32 v49, v1, v49\n"
    "v_cvt_pk_bf16_f32 v48, v48, v49\n"
    "v_mul_f32 v50, v2, v50\n"
    "v_mul_f32 v51, v3, v51\n"
    "s_setprio 2\n"
    "s_waitcnt lgkmcnt(4)\n"
    "v_mfma_f32_32x32x16_bf16 v[16:31], v[80:83], v[160:163], v[16:31]\n"
    "ds_read_b64_tr_b16 v[92:93], v127 offset:49152\n"
    "ds_read_b64_tr_b16 v[94:95], v127 offset:49664\n"
    "v_cvt_pk_bf16_f32 v49, v50, v51\n"
    "v_mul_f32 v52, v4, v52\n"
    "v_mul_f32 v53, v5, v53\n"
    "v_cvt_pk_bf16_f32 v50, v52, v53\n"
    "v_mfma_f32_16x16x32_bf16 v[104:107], v[116:119], v[80:83], v[104:107]\n"
    "v_mul_f32 v54, v6, v54\n"
    "v_mul_f32 v55, v7, v55\n"
    "s_waitcnt lgkmcnt(4)\n"
    "v_mfma_f32_32x32x16_bf16 v[16:31], v[84:87], v[164:167], v[16:31]\n"
    "ds_read_b64_tr_b16 v[80:81], v124 offset:57344\n"
    "ds_read_b64_tr_b16 v[82:83], v124 offset:57856\n"
    "s_bfe_u32 s46, s72, 0x30018\n"
    "s_add_u32 m0, s50, 0x1e000\n"
    "s_mul_i32 s46, s46, 0x38000\n"
    "buffer_load_dwordx4 v78, s[12:15], s46 offen lds\n"
    "v_cvt_pk_bf16_f32 v51, v54, v55\n"
    "v_mul_f32 v56, v8, v56\n"
    "v_mul_f32 v57, v9, v57\n"
    "v_cvt_pk_bf16_f32 v52, v56, v57\n"
    "s_waitcnt lgkmcnt(4)\n"
    "v_mfma_f32_32x32x16_bf16 v[16:31], v[88:91], v[168:171], v[16:31]\n"
    "ds_read_b64_tr_b16 v[84:85], v125 offset:57344\n"
    "ds_read_b64_tr_b16 v[86:87], v125 offset:57856\n"
    "v_mul_f32 v58, v10, v58\n"
    "v_mul_f32 v59, v11, v59\n"
    "v_cvt_pk_bf16_f32 v53, v58, v59\n"
    "v_mul_f32 v60, v12, v60\n"
    "s_waitcnt lgkmcnt(4)\n"
    "v_mfma_f32_32x32x16_bf16 v[16:31], v[92:95], v[172:175], v[16:31]\n"
    "ds_read_b64_tr_b16 v[88:89], v126 offset:57344\n"
    "ds_read_b64_tr_b16 v[90:91], v126 offset:57856\n"
    "s_xor_b32 s60, s73, 1\n"
    "s_mul_i32 s60, s60, 0xe000\n"
    "s_add_u32 m0, s50, 0\n"
    "s_add_u32 s46, s60, 0\n"
    "buffer_load_dwordx4 v249, s[24:27], s46 offen lds\n"
    "v_mul_f32 v61, v13, v61\n"
    "v_cvt_pk_bf16_f32 v54, v60, v61\n"
    "v_mul_f32 v62, v14, v62\n"
    "v_mul_f32 v63, v15, v63\n"
    "s_setprio 1\n"
    "s_waitcnt lgkmcnt(4)\n"
    "v_mfma_f32_32x32x16_bf16 v[16:31], v[80:83], v[176:179], v[16:31]\n"
    "ds_read_b64_tr_b16 v[92:93], v127 offset:57344\n"
    "ds_read_b64_tr_b16 v[94:95], v127 offset:57856\n"
    "v_cvt_pk_bf16_f32 v55, v62, v63\n"
    "s_mov_b64 s[16:17], s[20:21]\n"
    "s_lshl_b32 s18, s26, 4\n"
    "s_lshr_b32 s30, s26, 2\n"
    "s_mov_b32 s36, 0x77ff77ff\n"
    "v_mfma_f32_16x16x32_bf16 v[104:107], v[116:119], v[80:83], v[104:107]\n"
    "s_mov_b32 s37, 0x77ff77ff\n"
    "s_waitcnt lgkmcnt(4)\n"
    "v_mfma_f32_32x32x16_bf16 v[16:31], v[84:87], v[180:183], v[16:31]\n"
    "ds_read_b64_tr_b16 v[80:81], v124 offset:16384\n"
    "ds_read_b64_tr_b16 v[82:83], v124 offset:16896\n"
    "s_add_u32 m0, s50, 0x2000\n"
    "s_add_u32 s46, s60, 0x38000\n"
    "buffer_load_dwordx4 v249, s[24:27], s46 offen lds\n"
    "s_waitcnt vmcnt(12)\n"
    "s_barrier\n"
    "s_waitcnt lgkmcnt(4)\n"
    "v_mfma_f32_32x32x16_bf16 v[16:31], v[88:91], v[184:187], v[16:31]\n"
    "ds_read_b64_tr_b16 v[84:85], v125 offset:16384\n"
    "ds_read_b64_tr_b16 v[86:87], v125 offset:16896\n"
    "s_waitcnt lgkmcnt(4)\n"
    "v_mfma_f32_32x32x16_bf16 v[16:31], v[92:95], v[188:191], v[16:31]\n"
    "ds_read_b64_tr_b16 v[88:89], v126 offset:16384\n"
    "ds_read_b64_tr_b16 v[90:91], v126 offset:16896\n"
    "s_add_u32 m0, s50, 0x4000\n"
    "s_add_u32 s46, s60, 0x70000\n"
    "buffer_load_dwordx4 v249, s[24:27], s46 offen lds\n"
    "s_setprio 0\n"
    "s_waitcnt lgkmcnt(4)\n"
    "v_mfma_f32_32x32x16_bf16 v[16:31], v[80:83], v[192:195], v[16:31]\n"
    "ds_read_b64_tr_b16 v[92:93], v127 offset:16384\n"
    "ds_read_b64_tr_b16 v[94:95], v127 offset:16896\n"
    "v_mfma_f32_16x16x32_bf16 v[104:107], v[116:119], v[80:83], v[104:107]\n"
    "s_waitcnt lgkmcnt(4)\n"
    "v_mfma_f32_32x32x16_bf16 v[16:31], v[84:87], v[196:199], v[16:31]\n"
    "ds_read_b64_tr_b16 v[80:81], v124 offset:24576\n"
    "ds_read_b64_tr_b16 v[82:83], v124 offset:25088\n"
    "s_xor_b32 s60, s73, 2\n"
    "s_mul_i32 s60, s60, 0xe000\n"
    "s_add_u32 m0, s50, 0x6000\n"
    "s_add_u32 s46, s60, 0\n"
    "buffer_load_dwordx4 v249, s[24:27], s46 offen lds\n"
    "s_waitcnt lgkmcnt(4)\n"
    "v_mfma_f32_32x32x16_bf16 v[16:31], v[88:91], v[200:203], v[16:31]\n"
    "ds_read_b64_tr_b16 v[84:85], v124 offset:32768\n"
    "ds_read_b64_tr_b16 v[86:87], v124 offset:33280\n"
    "s_waitcnt lgkmcnt(4)\n"
    "v_mfma_f32_32x32x16_bf16 v[16:31], v[92:95], v[204:207], v[16:31]\n"
    "ds_read_b64_tr_b16 v[88:89], v125 offset:24576\n"
    "ds_read_b64_tr_b16 v[90:91], v125 offset:25088\n"
    "s_add_u32 m0, s50, 0x10000\n"
    "s_add_u32 s46, s60, 0x38000\n"
    "buffer_load_dwordx4 v249, s[24:27], s46 offen lds\n"
    "s_setprio 3\n"
    "s_waitcnt lgkmcnt(4)\n"
    "v_mfma_f32_32x32x16_bf16 v[16:31], v[80:83], v[208:211], v[16:31]\n"
    "ds_read_b64_tr_b16 v[92:93], v126 offset:24576\n"
    "ds_read_b64_tr_b16 v[94:95], v126 offset:25088\n"
    "v_mfma_f32_16x16x32_bf16 v[104:107], v[116:119], v[80:83], v[104:107]\n"
    "s_waitcnt lgkmcnt(4)\n"
    "v_mfma_f32_32x32x16_bf16 v[16:31], v[84:87], v[224:227], v[16:31]\n"
    "ds_read_b64_tr_b16 v[80:81], v127 offset:24576\n"
    "ds_read_b64_tr_b16 v[82:83], v127 offset:25088\n"
    "v_mfma_f32_16x16x32_bf16 v[104:107], v[116:119], v[84:87], v[104:107]\n"
    "s_waitcnt lgkmcnt(4)\n"
    "v_mfma_f32_32x32x16_bf16 v[16:31], v[88:91], v[212:215], v[16:31]\n"
    "ds_read_b64_tr_b16 v[84:85], v125 offset:32768\n"
    "ds_read_b64_tr_b16 v[86:87], v125 offset:33280\n"
    "s_mov_b32 s46, 0xf00000\n"
    "s_mov_b64 exec, 0xffff\n"
    "buffer_load_dwordx2 v[242:243], v250, s[20:23], s46 offen sc1\n"
    "s_mov_b64 exec, -1\n"
    "s_waitcnt lgkmcnt(4)\n"
    "v_mfma_f32_32x32x16_bf16 v[16:31], v[92:95], v[216:219], v[16:31]\n"
    "ds_read_b64_tr_b16 v[88:89], v126 offset:32768\n"
    "ds_read_b64_tr_b16 v[90:91], v126 offset:33280\n"
    "v_and_b32 v105, s71, v104\n"
    "v_sub_f32 v104, v104, v105\n"
    "v_lshrrev_b32 v104, 16, v104\n"
    "v_or_b32 v105, v105, v104\n"
    "ds_write_b32 v247, v105 offset:3072\n"
    "s_setprio 2\n"
    "s_waitcnt lgkmcnt(5)\n"
    "v_mfma_f32_32x32x16_bf16 v[16:31], v[80:83], v[220:223], v[16:31]\n"
    "ds_read_b64_tr_b16 v[92:93], v127 offset:32768\n"
    "ds_read_b64_tr_b16 v[94:95], v127 offset:33280\n"
    "s_waitcnt vmcnt(6) lgkmcnt(0)\n"
    "s_barrier\n"
    "v_mfma_f32_32x32x16_bf16 v[16:31], v[84:87], v[228:231], v[16:31]\n"
    "ds_read_b128 v[108:111], v248 offset:3072\n"
    "v_add_u32 v124, 0x14000, v124\n"
    "ds_read_b64_tr_b16 v[80:81], v124 offset:32768\n"
    "ds_read_b64_tr_b16 v[82:83], v124 offset:33280\n"
    "buffer_store_dwordx4 v[48:51], v244, s[20:23], s56 offen sc1\n"
    "v_mfma_f32_32x32x16_bf16 v[16:31], v[88:91], v[232:235], v[16:31]\n"
    "v_add_u32 v125, 0x14000, v125\n"
    "ds_read_b64_tr_b16 v[84:85], v125 offset:32768\n"
    "ds_read_b64_tr_b16 v[86:87], v125 offset:33280\n"
    "buffer_store_dwordx4 v[52:55], v244, s[20:23], s56 offen offset:1024 sc1\n"
    "v_mfma_f32_32x32x16_bf16 v[16:31], v[92:95], v[236:239], v[16:31]\n"
    "v_add_u32 v126, 0x14000, v126\n"
    "ds_read_b64_tr_b16 v[88:89], v126 offset:32768\n"
    "ds_read_b64_tr_b16 v[90:91], v126 offset:33280\n"
    "v_mov_b32 v48, 0x3f803f80\n"
    "v_mov_b32 v49, 0x3f803f80\n"
    "v_mov_b32 v50, 0x3f803f80\n"
    "v_mov_b32 v51, 0x3f803f80\n"
    "s_waitcnt lgkmcnt(6)\n"
    "v_mfma_f32_32x32x16_bf16 v[16:31], v[108:111], v[120:123], v[16:31]\n"
    "v_mov_b32 v52, 0x3f803f80\n"
    "v_mov_b32 v53, 0x3f803f80\n"
    "v_mov_b32 v54, 0x3f803f80\n"
    "v_mov_b32 v55, 0x3f803f80\n"
    "s_setprio 1\n"
    "s_waitcnt lgkmcnt(4)\n"
    "v_mfma_f32_32x32x16_bf16 v[64:79], v[80:83], v[128:131], 0\n"
    "v_add_u32 v127, 0x14000, v127\n"
    "ds_read_b64_tr_b16 v[92:93], v127 offset:32768\n"
    "ds_read_b64_tr_b16 v[94:95], v127 offset:33280\n"
    "s_add_u32 m0, s50, 0x12000\n"
    "s_add_u32 s46, s60, 0x70000\n"
    "buffer_load_dwordx4 v249, s[24:27], s46 offen lds\n"
    "v_mov_b32 v56, 0x3f803f80\n"
    "v_mov_b32 v57, 0x3f803f80\n"
    "v_mov_b32 v58, 0x3f803f80\n"
    "v_mov_b32 v59, 0x3f803f80\n"
    "v_mov_b32 v60, 0x3f803f80\n"
    "v_mfma_f32_16x16x32_bf16 v[104:107], v[116:119], v[80:83], 0\n"
    "v_mov_b32 v61, 0x3f803f80\n"
    "v_mov_b32 v62, 0x3f803f80\n"
    "s_waitcnt lgkmcnt(4)\n"
    "v_mfma_f32_32x32x16_bf16 v[64:79], v[84:87], v[132:135], v[64:79]\n"
    "ds_read_b64_tr_b16 v[80:81], v124 offset:40960\n"
    "ds_read_b64_tr_b16 v[82:83], v124 offset:41472\n"
    "v_mov_b32 v63, 0x3f803f80\n"
    "s_waitcnt lgkmcnt(4)\n"
    "v_mfma_f32_32x32x16_bf16 v[64:79], v[88:91], v[136:139], v[64:79]\n"
    "ds_read_b64_tr_b16 v[84:85], v125 offset:40960\n"
    "ds_read_b64_tr_b16 v[86:87], v125 offset:41472\n"
    "v_mul_f32 v100, v16, v16\n"
    "v_mul_f32 v101, v17, v17\n"
    "v_mul_f32 v102, v18, v18\n"
    "v_mul_f32 v103, v19, v19\n"
    "v_fma_f32 v100, v100, v97, v96\n"
    "s_waitcnt lgkmcnt(4)\n"
    "v_mfma_f32_32x32x16_bf16 v[64:79], v[92:95], v[140:143], v[64:79]\n"
    "ds_read_b64_tr_b16 v[88:89], v126 offset:40960\n"
    "ds_read_b64_tr_b16 v[90:91], v126 offset:41472\n"
    "v_fma_f32 v101, v101, v97, v96\n"
    "v_fma_f32 v102, v102, v97, v96\n"
    "v_fma_f32 v103, v103, v97, v96\n"
    "v_mul_f32 v100, v16, v100\n"
    "v_mul_f32 v101, v17, v101\n"
    "s_setprio 0\n"
    "s_waitcnt lgkmcnt(4)\n"
    "v_mfma_f32_32x32x16_bf16 v[64:79], v[80:83], v[144:147], v[64:79]\n"
    "ds_read_b64_tr_b16 v[92:93], v127 offset:40960\n"
    "ds_read_b64_tr_b16 v[94:95], v127 offset:41472\n"
    "s_mov_b32 s46, 0xf00000\n"
    "s_mov_b64 exec, 0xffff\n"
    "buffer_load_dwordx2 v[4:5], v250, s[20:23], s46 offen sc1\n"
    "s_mov_b64 exec, -1\n"
    "v_mul_f32 v102, v18, v102\n"
    "v_mul_f32 v103, v19, v103\n"
    "v_exp_f32 v100, v100\n"
    "v_exp_f32 v101, v101\n"
    "v_mfma_f32_16x16x32_bf16 v[104:107], v[116:119], v[80:83], v[104:107]\n"
    "v_exp_f32 v102, v102\n"
    "s_waitcnt lgkmcnt(4)\n"
    "v_mfma_f32_32x32x16_bf16 v[64:79], v[84:87], v[148:151], v[64:79]\n"
    "ds_read_b64_tr_b16 v[80:81], v124 offset:49152\n"
    "ds_read_b64_tr_b16 v[82:83], v124 offset:49664\n"
    "v_exp_f32 v103, v103\n"
    "v_fma_f32 v100, v100, v98, v98\n"
    "v_fma_f32 v101, v101, v98, v98\n"
    "s_waitcnt lgkmcnt(4)\n"
    "v_mfma_f32_32x32x16_bf16 v[64:79], v[88:91], v[152:155], v[64:79]\n"
    "ds_read_b64_tr_b16 v[84:85], v125 offset:49152\n"
    "ds_read_b64_tr_b16 v[86:87], v125 offset:49664\n"
    "v_fma_f32 v102, v102, v98, v98\n"
    "v_fma_f32 v103, v103, v98, v98\n"
    "v_rcp_f32 v100, v100\n"
    "v_rcp_f32 v101, v101\n"
    "s_waitcnt lgkmcnt(4)\n"
    "v_mfma_f32_32x32x16_bf16 v[64:79], v[92:95], v[156:159], v[64:79]\n"
    "s_waitcnt vmcnt(4)\n"
    "v_cmp_ne_u64 vcc, v[242:243], v[240:241]\n"
    "s_nop 1\n"
    "s_and_b32 s45, vcc_lo, 0xffff\n"
    "s_cselect_b32 s52, 1, 0\n"
    "ds_read_b64_tr_b16 v[88:89], v126 offset:49152\n"
    "ds_read_b64_tr_b16 v[90:91], v126 offset:49664\n"
    "buffer_load_dwordx4 v[0:3], v245, s[20:23], s65 offen sc1\n"
    "v_rcp_f32 v102, v102\n"
    "v_rcp_f32 v103, v103\n"
    "s_setprio 3\n"
    "s_waitcnt lgkmcnt(4)\n"
    "v_mfma_f32_32x32x16_bf16 v[64:79], v[80:83], v[160:163], v[64:79]\n"
    "ds_read_b64_tr_b16 v[92:93], v127 offset:49152\n"
    "ds_read_b64_tr_b16 v[94:95], v127 offset:49664\n"
    "v_mul_f32 v16, v16, v100\n"
    "v_mul_f32 v17, v17, v101\n"
    "v_mul_f32 v18, v18, v102\n"
    "v_mul_f32 v19, v19, v103\n"
    "v_mul_f32 v100, v20, v20\n"
    "v_mfma_f32_16x16x32_bf16 v[104:107], v[116:119], v[80:83], v[104:107]\n"
    "v_mul_f32 v101, v21, v21\n"
    "v_mul_f32 v102, v22, v22\n"
    "s_waitcnt lgkmcnt(4)\n"
    "v_mfma_f32_32x32x16_bf16 v[64:79], v[84:87], v[164:167], v[64:79]\n"
    "ds_read_b64_tr_b16 v[80:81], v124\n"
    "ds_read_b64_tr_b16 v[82:83], v124 offset:512\n"
    "buffer_load_dwordx4 v[32:35], v245, s[20:23], s61 offen sc1\n"
    "v_mul_f32 v103, v23, v23\n"
    "v_fma_f32 v100, v100, v97, v96\n"
    "v_fma_f32 v101, v101, v97, v96\n"
    "v_fma_f32 v102, v102, v97, v96\n"
    "v_fma_f32 v103, v103, v97, v96\n"
    "s_waitcnt lgkmcnt(4)\n"
    "v_mfma_f32_32x32x16_bf16 v[64:79], v[88:91], v[168:171], v[64:79]\n"
    "ds_read_b64_tr_b16 v[84:85], v125\n"
    "ds_read_b64_tr_b16 v[86:87], v125 offset:512\n"
    "v_mul_f32 v100, v20, v100\n"
    "v_mul_f32 v101, v21, v101\n"
    "v_mul_f32 v102, v22, v102\n"
    "v_mul_f32 v103, v23, v103\n"
    "v_exp_f32 v100, v100\n"
    "s_waitcnt lgkmcnt(4)\n"
    "v_mfma_f32_32x32x16_bf16 v[64:79], v[92:95], v[172:175], v[64:79]\n"
    "ds_read_b64_tr_b16 v[88:89], v126\n"
    "ds_read_b64_tr_b16 v[90:91], v126 offset:512\n"
    "buffer_load_dwordx4 v[36:39], v245, s[20:23], s62 offen sc1\n"
    "v_exp_f32 v101, v101\n"
    "v_exp_f32 v102, v102\n"
    "s_setprio 2\n"
    "s_waitcnt lgkmcnt(4)\n"
    "v_mfma_f32_32x32x16_bf16 v[64:79], v[80:83], v[176:179], v[64:79]\n"
    "ds_read_b64_tr_b16 v[92:93], v127\n"
    "ds_read_b64_tr_b16 v[94:95], v127 offset:512\n"
    "s_cmp_eq_u32 s52, 1\n"
    "s_cbranch_scc0 .Lk_if11\n"
    "s_waitcnt vmcnt(3)\n"
    "v_cmp_ne_u64 vcc, v[4:5], v[240:241]\n"
    "s_nop 1\n"
    "s_and_b32 s45, vcc_lo, 0xffff\n"
    "s_cselect_b32 s52, 1, 0\n"
    "s_waitcnt vmcnt(2)\n"
    "buffer_load_dwordx4 v[0:3], v245, s[20:23], s65 offen sc1\n"
    "s_waitcnt vmcnt(2)\n"
    "buffer_load_dwordx4 v[32:35], v245, s[20:23], s61 offen sc1\n"
    "s_waitcnt vmcnt(2)\n"
    "buffer_load_dwordx4 v[36:39], v245, s[20:23], s62 offen sc1\n"
    ".Lk_if11:\n"
    "s_waitcnt vmcnt(5)\n"
    "s_mov_b64 exec, 1\n"
    "buffer_store_dwordx2 v[240:241], off, s[20:23], s59 sc1\n"
    "s_mov_b64 exec, -1\n"
    "v_exp_f32 v103, v103\n"
    "v_fma_f32 v100, v100, v98, v98\n"
    "v_fma_f32 v101, v101, v98, v98\n"
    "v_fma_f32 v102, v102, v98, v98\n"
    "v_mfma_f32_16x16x32_bf16 v[104:107], v[116:119], v[80:83], v[104:107]\n"
    "v_fma_f32 v103, v103, v98, v98\n"
    "v_rcp_f32 v100, v100\n"
    "s_waitcnt lgkmcnt(4)\n"
    "v_mfma_f32_32x32x16_bf16 v[64:79], v[84:87], v[180:183], v[64:79]\n"
    "ds_read_b64_tr_b16 v[80:81], v124 offset:8192\n"
    "ds_read_b64_tr_b16 v[82:83], v124 offset:8704\n"
    "s_xor_b32 s47, s63, 0\n"
    "s_mov_b64 exec, s[36:37]\n"
    "buffer_load_dwordx4 v[48:51], v254, s[20:23], s47 offen sc1\n"
    "buffer_load_dwordx4 v[52:55], v254, s[20:23], s47 offen offset:2048 sc1\n"
    "s_mov_b64 exec, -1\n"
    "v_rcp_f32 v101, v101\n"
    "v_rcp_f32 v102, v102\n"
    "s_waitcnt lgkmcnt(4)\n"
    "v_mfma_f32_32x32x16_bf16 v[64:79], v[88:91], v[184:187], v[64:79]\n"
    "ds_read_b64_tr_b16 v[84:85], v125 offset:8192\n"
    "ds_read_b64_tr_b16 v[86:87], v125 offset:8704\n"
    "v_rcp_f32 v103, v103\n"
    "v_mul_f32 v20, v20, v100\n"
    "v_mul_f32 v21, v21, v101\n"
    "v_mul_f32 v22, v22, v102\n"
    "s_waitcnt lgkmcnt(4)\n"
    "v_mfma_f32_32x32x16_bf16 v[64:79], v[92:95], v[188:191], v[64:79]\n"
    "ds_read_b64_tr_b16 v[88:89], v126 offset:8192\n"
    "ds_read_b64_tr_b16 v[90:91], v126 offset:8704\n"
    "v_mul_f32 v23, v23, v103\n"
    "v_mul_f32 v100, v24, v24\n"
    "v_mul_f32 v101, v25, v25\n"
    "v_mul_f32 v102, v26, v26\n"
    "v_mul_f32 v103, v27, v27\n"
    "s_setprio 1\n"
    "s_waitcnt lgkmcnt(4)\n"
    "v_mfma_f32_32x32x16_bf16 v[64:79], v[80:83], v[192:195], v[64:79]\n"
    "ds_read_b64_tr_b16 v[92:93], v127 offset:8192\n"
    "ds_read_b64_tr_b16 v[94:95], v127 offset:8704\n"
    "v_fma_f32 v100, v100, v97, v96\n"
    "v_fma_f32 v101, v101, v97, v96\n"
    "v_fma_f32 v102, v102, v97, v96\n"
    "v_fma_f32 v103, v103, v97, v96\n"
    "v_mul_f32 v100, v24, v100\n"
    "v_mfma_f32_16x16x32_bf16 v[104:107], v[116:119], v[80:83], v[104:107]\n"
    "v_mul_f32 v101, v25, v101\n"
    "v_mul_f32 v102, v26, v102\n"
    "s_waitcnt lgkmcnt(4)\n"
    "v_mfma_f32_32x32x16_bf16 v[64:79], v[84:87], v[196:199], v[64:79]\n"
    "ds_read_b64_tr_b16 v[80:81], v124 offset:16384\n"
    "ds_read_b64_tr_b16 v[82:83], v124 offset:16896\n"
    "s_xor_b32 s47, s61, 0x200\n"
    "buffer_load_dwordx4 v[40:43], v245, s[20:23], s47 offen sc1\n"
    "v_mul_f32 v103, v27, v103\n"
    "v_exp_f32 v100, v100\n"
    "v_exp_f32 v101, v101\n"
    "s_waitcnt lgkmcnt(4)\n"
    "v_mfma_f32_32x32x16_bf16 v[64:79], v[88:91], v[200:203], v[64:79]\n"
    "ds_read_b64_tr_b16 v[84:85], v124 offset:24576\n"
    "ds_read_b64_tr_b16 v[86:87], v124 offset:25088\n"
    "v_exp_f32 v102, v102\n"
    "v_exp_f32 v103, v103\n"
    "v_fma_f32 v100, v100, v98, v98\n"
    "s_waitcnt lgkmcnt(4)\n"
    "v_mfma_f32_32x32x16_bf16 v[64:79], v[92:95], v[204:207], v[64:79]\n"
    "ds_read_b64_tr_b16 v[88:89], v125 offset:16384\n"
    "ds_read_b64_tr_b16 v[90:91], v125 offset:16896\n"
    "s_xor_b32 s47, s62, 0x200\n"
    "buffer_load_dwordx4 v[44:47], v245, s[20:23], s47 offen sc1\n"
    "v_fma_f32 v101, v101, v98, v98\n"
    "v_fma_f32 v102, v102, v98, v98\n"
    "v_fma_f32 v103, v103, v98, v98\n"
    "v_rcp_f32 v100, v100\n"
    "s_setprio 0\n"
    "s_waitcnt lgkmcnt(4)\n"
    "v_mfma_f32_32x32x16_bf16 v[64:79], v[80:83], v[208:211], v[64:79]\n"
    "ds_read_b64_tr_b16 v[92:93], v126 offset:16384\n"
    "ds_read_b64_tr_b16 v[94:95], v126 offset:16896\n"
    "v_rcp_f32 v101, v101\n"
    "v_rcp_f32 v102, v102\n"
    "v_rcp_f32 v103, v103\n"
    "v_mfma_f32_16x16x32_bf16 v[104:107], v[116:119], v[80:83], v[104:107]\n"
    "v_mul_f32 v24, v24, v100\n"
    "s_waitcnt lgkmcnt(4)\n"
    "v_mfma_f32_32x32x16_bf16 v[64:79], v[84:87], v[224:227], v[64:79]\n"
    "ds_read_b64_tr_b16 v[80:81], v127 offset:16384\n"
    "ds_read_b64_tr_b16 v[82:83], v127 offset:16896\n"
    "s_xor_b32 s47, s63, 0x1000\n"
    "s_mov_b64 exec, s[36:37]\n"
    "buffer_load_dwordx4 v[56:59], v254, s[20:23], s47 offen sc1\n"
    "buffer_load_dwordx4 v[60:63], v254, s[20:23], s47 offen offset:2048 sc1\n"
    "s_mov_b64 exec, -1\n"
    "v_mul_f32 v25, v25, v101\n"
    "v_mul_f32 v26, v26, v102\n"
    "v_mul_f32 v27, v27, v103\n"
    "v_mul_f32 v100, v28, v28\n"
    "v_mul_f32 v101, v29, v29\n"
    "v_mfma_f32_16x16x32_bf16 v[104:107], v[116:119], v[84:87], v[104:107]\n"
    "v_mul_f32 v102, v30, v30\n"
    "v_mul_f32 v103, v31, v31\n"
    "s_waitcnt lgkmcnt(4)\n"
    "v_mfma_f32_32x32x16_bf16 v[64:79], v[88:91], v[212:215], v[64:79]\n"
    "ds_read_b64_tr_b16 v[84:85], v125 offset:24576\n"
    "ds_read_b64_tr_b16 v[86:87], v125 offset:25088\n"
    "v_fma_f32 v100, v100, v97, v96\n"
    "v_fma_f32 v101, v101, v97, v96\n"
    "v_fma_f32 v102, v102, v97, v96\n"
    "v_fma_f32 v103, v103, v97, v96\n"
    "v_mul_f32 v100, v28, v100\n"
    "s_waitcnt lgkmcnt(4)\n"
    "v_mfma_f32_32x32x16_bf16 v[64:79], v[92:95], v[216:219], v[64:79]\n"
    "ds_read_b64_tr_b16 v[88:89], v126 offset:24576\n"
    "ds_read_b64_tr_b16 v[90:91], v126 offset:25088\n"
    "v_and_b32 v105, s71, v104\n"
    "v_sub_f32 v104, v104, v105\n"
    "v_lshrrev_b32 v104, 16, v104\n"
    "v_or_b32 v105, v105, v104\n"
    "ds_write_b32 v247, v105\n"
    "s_setprio 3\n"
    "s_waitcnt lgkmcnt(5)\n"
    "v_mfma_f32_32x32x16_bf16 v[64:79], v[80:83], v[220:223], v[64:79]\n"
    "ds_read_b64_tr_b16 v[92:93], v127 offset:24576\n"
    "ds_read_b64_tr_b16 v[94:95], v127 offset:25088\n"
    "v_mul_f32 v101, v29, v101\n"
    "v_mul_f32 v102, v30, v102\n"
    "v_mul_f32 v103, v31, v103\n"
    "v_exp_f32 v100, v100\n"
    "s_cmp_eq_u32 s52, 1\n"
    "s_cbranch_scc0 .Lk_if13\n"
    "s_mov_b32 s70, 0x40000\n"
    ".Lk_lp12:\n"
    "s_sleep 1\n"
    "s_mov_b32 s46, 0xf00000\n"
    "s_mov_b64 exec, 0xffff\n"
    "buffer_load_dwordx2 v[242:243], v250, s[20:23], s46 offen sc1\n"
    "s_mov_b64 exec, -1\n"
    "s_waitcnt vmcnt(0)\n"
    "v_cmp_ne_u64 vcc, v[242:243], v[240:241]\n"
    "s_sub_u32 s70, s70, 1\n"
    "s_nop 0\n"
    "s_cselect_b64 s[38:39], 0, vcc\n"
    "s_and_b32 s45, s38, 0xffff\n"
    "s_cbranch_scc1 .Lk_lp12\n"
    "buffer_load_dwordx4 v[0:3], v245, s[20:23], s65 offen sc1\n"
    "buffer_load_dwordx4 v[32:35], v245, s[20:23], s61 offen sc1\n"
    "buffer_load_dwordx4 v[36:39], v245, s[20:23], s62 offen sc1\n"
    "s_xor_b32 s47, s63, 0\n"
    "s_mov_b64 exec, s[36:37]\n"
    "buffer_load_dwordx4 v[48:51], v254, s[20:23], s47 offen sc1\n"
    "buffer_load_dwordx4 v[52:55], v254, s[20:23], s47 offen offset:2048 sc1\n"
    "s_mov_b64 exec, -1\n"
    "s_xor_b32 s47, s61, 0x200\n"
    "buffer_load_dwordx4 v[40:43], v245, s[20:23], s47 offen sc1\n"
    "s_xor_b32 s47, s62, 0x200\n"
    "buffer_load_dwordx4 v[44:47], v245, s[20:23], s47 offen sc1\n"
    "s_xor_b32 s47, s63, 0x1000\n"
    "s_mov_b64 exec, s[36:37]\n"
    "buffer_load_dwordx4 v[56:59], v254, s[20:23], s47 offen sc1\n"
    "buffer_load_dwordx4 v[60:63], v254, s[20:23], s47 offen offset:2048 sc1\n"
    "s_mov_b64 exec, -1\n"
    "s_waitcnt vmcnt(0)\n"
    ".Lk_if13:\n"
    "s_lshr_b32 s45, s40, 2\n"
    "s_xor_b32 s45, s45, s41\n"
    "s_add_u32 s47, s50, 0xa000\n"
    "v_add_u32 v246, s47, v244\n"
    "s_bitcmp1_b32 s45, 0\n"
    "s_cbranch_scc0 .Lk_if15\n"
    "s_bitcmp1_b32 s41, 0\n"
    "s_cbranch_scc0 .Lk_if14\n"
    "s_mov_b32 exec_hi, 0\n"
    "s_waitcnt vmcnt(9)\n"
    "v_mov_b32 v0, 0\n"
    "v_mov_b32 v1, 0\n"
    "s_mov_b64 exec, -1\n"
    "s_branch .Lk_if14_end\n"
    ".Lk_if14:\n"
    "s_mov_b32 exec_lo, 0\n"
    "s_waitcnt vmcnt(9)\n"
    "v_mov_b32 v2, 0\n"
    "v_mov_b32 v3, 0\n"
    "s_mov_b64 exec, -1\n"
    ".Lk_if14_end:\n"
    ".Lk_if15:\n"
    "s_waitcnt vmcnt(9)\n"
    "ds_write_b128 v246, v[0:3]\n"
    "v_and_b32 v246, 0xffffefff, v246\n"
    "s_waitcnt lgkmcnt(0)\n"
    "s_barrier\n"
    "v_mfma_f32_32x32x16_bf16 v[64:79], v[84:87], v[228:231], v[64:79]\n"
    "ds_read_b128 v[108:111], v248\n"
    "v_xor_b32 v250, 0x400, v246\n"
    "ds_read_b128 v[112:115], v250\n"
    "ds_read_b128 v[116:119], v250 offset:4096\n"
    "v_exp_f32 v101, v101\n"
    "v_exp_f32 v102, v102\n"
    "v_exp_f32 v103, v103\n"
    "v_mfma_f32_32x32x16_bf16 v[64:79], v[88:91], v[232:235], v[64:79]\n"
    "v_fma_f32 v100, v100, v98, v98\n"
    "v_fma_f32 v101, v101, v98, v98\n"
    "v_fma_f32 v102, v102, v98, v98\n"
    "v_fma_f32 v103, v103, v98, v98\n"
    "v_mfma_f32_32x32x16_bf16 v[64:79], v[92:95], v[236:239], v[64:79]\n"
    "v_rcp_f32 v100, v100\n"
    "v_rcp_f32 v101, v101\n"
    "v_rcp_f32 v102, v102\n"
    "s_waitcnt lgkmcnt(2)\n"
    "v_mfma_f32_32x32x16_bf16 v[64:79], v[108:111], v[120:123], v[64:79]\n"
    "v_rcp_f32 v103, v103\n"
    "v_mul_f32 v28, v28, v100\n"
    "v_mul_f32 v29, v29, v101\n"
    "v_mul_f32 v30, v30, v102\n"
    "v_mul_f32 v31, v31, v103\n"
    "s_waitcnt vmcnt(8) lgkmcnt(1)\n"
    "v_mfma_f32_32x32x16_bf16 v[0:15], v[32:35], v[112:115], 0\n"
    "v_xor_b32 v250, 0x800, v246\n"
    "ds_read_b128 v[120:123], v250\n"
    "ds_read_b128 v[124:127], v250 offset:4096\n"
    "s_waitcnt vmcnt(7) lgkmcnt(2)\n"
    "v_mfma_f32_32x32x16_bf16 v[0:15], v[36:39], v[116:119], v[0:15]\n"
    "v_xor_b32 v250, 0xc00, v246\n"
    "ds_read_b128 v[224:227], v250\n"
    "ds_read_b128 v[228:231], v250 offset:4096\n"
    "ds_read_b128 v[232:235], v246\n"
    "ds_read_b128 v[236:239], v246 offset:4096\n"
    "s_mov_b64 exec, 0xffff\n"
    "buffer_load_dwordx2 v[242:243], v253, s[20:23], s66 offen sc1\n"
    "s_mov_b64 exec, -1\n"
    "v_mul_f32 v64, v16, v64\n"
    "v_mul_f32 v65, v17, v65\n"
    "v_cvt_pk_bf16_f32 v64, v64, v65\n"
    "v_mul_f32 v66, v18, v66\n"
    "v_mul_f32 v67, v19, v67\n"
    "v_cvt_pk_bf16_f32 v65, v66, v67\n"
    "v_mul_f32 v68, v20, v68\n"
    "v_mul_f32 v69, v21, v69\n"
    "v_cvt_pk_bf16_f32 v66, v68, v69\n"
    "v_mul_f32 v70, v22, v70\n"
    "v_mul_f32 v71, v23, v71\n"
    "v_cvt_pk_bf16_f32 v67, v70, v71\n"
    "buffer_store_dwordx4 v[64:67], v244, s[20:23], s57 offen sc1\n"
    "v_mul_f32 v72, v24, v72\n"
    "v_mul_f32 v73, v25, v73\n"
    "v_cvt_pk_bf16_f32 v68, v72, v73\n"
    "v_mul_f32 v74, v26, v74\n"
    "v_mul_f32 v75, v27, v75\n"
    "v_cvt_pk_bf16_f32 v69, v74, v75\n"
    "v_mul_f32 v76, v28, v76\n"
    "v_mul_f32 v77, v29, v77\n"
    "v_cvt_pk_bf16_f32 v70, v76, v77\n"
    "v_mul_f32 v78, v30, v78\n"
    "v_mul_f32 v79, v31, v79\n"
    "v_cvt_pk_bf16_f32 v71, v78, v79\n"
    "buffer_store_dwordx4 v[68:71], v244, s[20:23], s57 offen offset:1024 sc1\n"
    "s_branch .Lk_if22_end\n"
    ".Lk_if22:\n"
    "s_lshr_b32 s45, s40, 2\n"
    "s_lshl1_add_u32 s45, s45, 2\n"
    "s_cmp_eq_u32 s45, s52\n"
    "s_cbranch_scc0 .Lk_if16\n"
    "s_setprio 3\n"
    "s_waitcnt lgkmcnt(5)\n"
    "v_mfma_f32_32x32x16_bf16 v[48:63], v[80:83], v[128:131], 0\n"
    "ds_read_b64_tr_b16 v[92:93], v127 offset:32768\n"
    "ds_read_b64_tr_b16 v[94:95], v127 offset:33280\n"
    "s_bfe_u32 s46, s72, 0x30010\n"
    "s_add_u32 m0, s50, 0x8000\n"
    "s_mul_i32 s46, s46, 0x38000\n"
    "buffer_load_dwordx4 v78, s[8:11], s46 offen lds\n"
    "v_add_f32 v108, v108, v110\n"
    "v_add_f32 v112, v112, v114\n"
    "v_add_f32 v109, v109, v111\n"
    "v_add_f32 v113, v113, v115\n"
    "v_add_f32 v108, v108, v112\n"
    "v_add_f32 v109, v109, v113\n"
    "v_mfma_f32_16x16x32_bf16 v[104:107], v[116:119], v[80:83], 0\n"
    "v_mul_f32 v108, 0x3b124925, v108\n"
    "v_cmp_gt_u32 vcc, 0x200, v244\n"
    "s_waitcnt lgkmcnt(5)\n"
    "v_mfma_f32_32x32x16_bf16 v[48:63], v[84:87], v[132:135], v[48:63]\n"
    "ds_read_b64_tr_b16 v[80:81], v124 offset:40960\n"
    "ds_read_b64_tr_b16 v[82:83], v124 offset:41472\n"
    "v_mul_f32 v109, 0x3b124925, v109\n"
    "v_cvt_pk_bf16_f32 v110, -v108, -v108\n"
    "v_fma_f32 v109, -v108, v108, v109\n"
    "v_cndmask_b32 v120, 0, v110, vcc\n"
    "v_mov_b32 v121, v120\n"
    "v_mov_b32 v122, v120\n"
    "s_waitcnt lgkmcnt(4)\n"
    "v_mfma_f32_32x32x16_bf16 v[48:63], v[88:91], v[136:139], v[48:63]\n"
    "ds_read_b64_tr_b16 v[84:85], v125 offset:40960\n"
    "ds_read_b64_tr_b16 v[86:87], v125 offset:41472\n"
    "v_mov_b32 v123, v120\n"
    "s_nop 1\n"
    "v_mfma_f32_32x32x16_bf16 v[32:47], v[24:27], v[120:123], v[32:47]\n"
    "s_cmp_eq_u32 s52, 8\n"
    "s_cselect_b32 s48, 1.0, 0x3e8b97f6\n"
    "v_max_f32 v109, 0, v109\n"
    "v_cvt_pk_bf16_f32 v109, 0, v109\n"
    "s_waitcnt lgkmcnt(4)\n"
    "v_mfma_f32_32x32x16_bf16 v[48:63], v[92:95], v[140:143], v[48:63]\n"
    "ds_read_b64_tr_b16 v[88:89], v126 offset:40960\n"
    "ds_read_b64_tr_b16 v[90:91], v126 offset:41472\n"
    "v_add_f32 v109, 0x3727c5ac, v109\n"
    "v_cvt_pk_bf16_f32 v109, 0, v109\n"
    "v_sqrt_f32 v109, v109\n"
    "v_add_u32 v240, 1, v240\n"
    "v_cvt_pk_bf16_f32 v109, 0, v109\n"
    "s_setprio 2\n"
    "s_waitcnt lgkmcnt(4)\n"
    "v_mfma_f32_32x32x16_bf16 v[48:63], v[80:83], v[144:147], v[48:63]\n"
    "ds_read_b64_tr_b16 v[92:93], v127 offset:40960\n"
    "ds_read_b64_tr_b16 v[94:95], v127 offset:41472\n"
    "v_rcp_f32 v99, v109\n"
    "v_mfma_f32_32x32x16_bf16 v[0:15], v[28:31], v[120:123], v[0:15]\n"
    "v_mul_f32 v98, v109, v109\n"
    "v_mul_f32 v100, s48, v99\n"
    "v_mfma_f32_16x16x32_bf16 v[104:107], v[116:119], v[80:83], v[104:107]\n"
    "v_mul_f32 v32, v100, v32\n"
    "v_mul_f32 v33, v100, v33\n"
    "s_waitcnt lgkmcnt(4)\n"
    "v_mfma_f32_32x32x16_bf16 v[48:63], v[84:87], v[148:151], v[48:63]\n"
    "ds_read_b64_tr_b16 v[80:81], v124 offset:49152\n"
    "ds_read_b64_tr_b16 v[82:83], v124 offset:49664\n"
    "v_cvt_pk_bf16_f32 v32, v32, v33\n"
    "v_mul_f32 v34, v100, v34\n"
    "v_mul_f32 v35, v100, v35\n"
    "v_cvt_pk_bf16_f32 v33, v34, v35\n"
    "v_mul_f32 v36, v100, v36\n"
    "v_mul_f32 v37, v100, v37\n"
    "s_waitcnt lgkmcnt(4)\n"
    "v_mfma_f32_32x32x16_bf16 v[48:63], v[88:91], v[152:155], v[48:63]\n"
    "ds_read_b64_tr_b16 v[84:85], v125 offset:49152\n"
    "ds_read_b64_tr_b16 v[86:87], v125 offset:49664\n"
    "v_cvt_pk_bf16_f32 v34, v36, v37\n"
    "v_mul_f32 v38, v100, v38\n"
    "v_mul_f32 v39, v100, v39\n"
    "v_cvt_pk_bf16_f32 v35, v38, v39\n"
    "v_mul_f32 v40, v100, v40\n"
    "v_mul_f32 v41, v100, v41\n"
    "s_waitcnt lgkmcnt(4)\n"
    "v_mfma_f32_32x32x16_bf16 v[48:63], v[92:95], v[156:159], v[48:63]\n"
    "ds_read_b64_tr_b16 v[88:89], v126 offset:49152\n"
    "ds_read_b64_tr_b16 v[90:91], v126 offset:49664\n"
    "v_cvt_pk_bf16_f32 v36, v40, v41\n"
    "v_mul_f32 v42, v100, v42\n"
    "v_mul_f32 v43, v100, v43\n"
    "v_cvt_pk_bf16_f32 v37, v42, v43\n"
    "v_mul_f32 v44, v100, v44\n"
    "v_mul_f32 v45, v100, v45\n"
    "s_setprio 1\n"
    "s_waitcnt lgkmcnt(4)\n"
    "v_mfma_f32_32x32x16_bf16 v[48:63], v[80:83], v[160:163], v[48:63]\n"
    "ds_read_b64_tr_b16 v[92:93], v127 offset:49152\n"
    "ds_read_b64_tr_b16 v[94:95], v127 offset:49664\n"
    "v_cvt_pk_bf16_f32 v38, v44, v45\n"
    "v_mul_f32 v46, v100, v46\n"
    "v_mul_f32 v47, v100, v47\n"
    "v_cvt_pk_bf16_f32 v39, v46, v47\n"
    "v_mfma_f32_16x16x32_bf16 v[104:107], v[116:119], v[80:83], v[104:107]\n"
    "v_permlane32_swap_b32 v32, v34\n"
    "v_permlane32_swap_b32 v33, v35\n"
    "v_permlane32_swap_b32 v36, v38\n"
    "v_permlane32_swap_b32 v37, v39\n"
    "s_add_u32 s47, s56, 0x10000\n"
    "buffer_store_dwordx4 v[32:35], v245, s[20:23], s56 offen sc1\n"
    "buffer_store_dwordx4 v[36:39], v245, s[20:23], s47 offen sc1\n"
    "s_add_u32 s56, s56, 0x40000\n"
    "s_waitcnt lgkmcnt(4)\n"
    "v_mfma_f32_32x32x16_bf16 v[48:63], v[84:87], v[164:167], v[48:63]\n"
    "ds_read_b64_tr_b16 v[80:81], v124\n"
    "ds_read_b64_tr_b16 v[82:83], v124 offset:512\n"
    "v_mul_f32 v0, v100, v0\n"
    "v_mul_f32 v1, v100, v1\n"
    "v_cvt_pk_bf16_f32 v0, v0, v1\n"
    "s_waitcnt lgkmcnt(4)\n"
    "v_mfma_f32_32x32x16_bf16 v[48:63], v[88:91], v[168:171], v[48:63]\n"
    "ds_read_b64_tr_b16 v[84:85], v125\n"
    "ds_read_b64_tr_b16 v[86:87], v125 offset:512\n"
    "v_mul_f32 v2, v100, v2\n"
    "v_mul_f32 v3, v100, v3\n"
    "v_cvt_pk_bf16_f32 v1, v2, v3\n"
    "v_mul_f32 v4, v100, v4\n"
    "v_mul_f32 v5, v100, v5\n"
    "v_cvt_pk_bf16_f32 v2, v4, v5\n"
    "s_waitcnt lgkmcnt(4)\n"
    "v_mfma_f32_32x32x16_bf16 v[48:63], v[92:95], v[172:175], v[48:63]\n"
    "ds_read_b64_tr_b16 v[88:89], v126\n"
    "ds_read_b64_tr_b16 v[90:91], v126 offset:512\n"
    "v_mul_f32 v6, v100, v6\n"
    "v_mul_f32 v7, v100, v7\n"
    "v_cvt_pk_bf16_f32 v3, v6, v7\n"
    "v_mul_f32 v8, v100, v8\n"
    "v_mul_f32 v9, v100, v9\n"
    "v_cvt_pk_bf16_f32 v4, v8, v9\n"
    "s_waitcnt vmcnt(7)\n"
    "s_barrier\n"
    "s_setprio 0\n"
    "s_waitcnt lgkmcnt(4)\n"
    "v_mfma_f32_32x32x16_bf16 v[48:63], v[80:83], v[176:179], v[48:63]\n"
    "ds_read_b64_tr_b16 v[92:93], v127\n"
    "ds_read_b64_tr_b16 v[94:95], v127 offset:512\n"
    "v_mul_f32 v10, v100, v10\n"
    "v_mul_f32 v11, v100, v11\n"
    "v_cvt_pk_bf16_f32 v5, v10, v11\n"
    "v_mul_f32 v12, v100, v12\n"
    "v_mul_f32 v13, v100, v13\n"
    "v_cvt_pk_bf16_f32 v6, v12, v13\n"
    "v_mfma_f32_16x16x32_bf16 v[104:107], v[116:119], v[80:83], v[104:107]\n"
    "v_mul_f32 v14, v100, v14\n"
    "v_mul_f32 v15, v100, v15\n"
    "s_waitcnt lgkmcnt(4)\n"
    "v_mfma_f32_32x32x16_bf16 v[48:63], v[84:87], v[180:183], v[48:63]\n"
    "ds_read_b64_tr_b16 v[80:81], v124 offset:8192\n"
    "ds_read_b64_tr_b16 v[82:83], v124 offset:8704\n"
    "s_bfe_u32 s46, s72, 0x30014\n"
    "s_add_u32 m0, s50, 0xa000\n"
    "s_mul_i32 s46, s46, 0x38000\n"
    "buffer_load_dwordx4 v78, s[8:11], s46 offen lds\n"
    "v_cvt_pk_bf16_f32 v7, v14, v15\n"
    "v_permlane32_swap_b32 v0, v2\n"
    "v_permlane32_swap_b32 v1, v3\n"
    "v_permlane32_swap_b32 v4, v6\n"
    "v_permlane32_swap_b32 v5, v7\n"
    "s_add_u32 s47, s56, 0x10000\n"
    "buffer_store_dwordx4 v[0:3], v245, s[20:23], s56 offen sc1\n"
    "buffer_store_dwordx4 v[4:7], v245, s[20:23], s47 offen sc1\n"
    "s_waitcnt lgkmcnt(4)\n"
    "v_mfma_f32_32x32x16_bf16 v[48:63], v[88:91], v[184:187], v[48:63]\n"
    "ds_read_b64_tr_b16 v[84:85], v125 offset:8192\n"
    "ds_read_b64_tr_b16 v[86:87], v125 offset:8704\n"
    "v_mul_f32 v96, 0xc0135761, v99\n"
    "v_mul_f32 v108, v99, v99\n"
    "v_mul_f32 v97, 0x3d372713, v96\n"
    "v_mul_f32 v97, v97, v108\n"
    "s_waitcnt lgkmcnt(4)\n"
    "v_mfma_f32_32x32x16_bf16 v[48:63], v[92:95], v[188:191], v[48:63]\n"
    "ds_read_b64_tr_b16 v[88:89], v126 offset:8192\n"
    "ds_read_b64_tr_b16 v[90:91], v126 offset:8704\n"
    "s_bfe_u32 s46, s72, 0x30018\n"
    "s_add_u32 m0, s50, 0xc000\n"
    "s_mul_i32 s46, s46, 0x38000\n"
    "buffer_load_dwordx4 v78, s[8:11], s46 offen lds\n"
    "s_setprio 3\n"
    "s_waitcnt lgkmcnt(4)\n"
    "v_mfma_f32_32x32x16_bf16 v[48:63], v[80:83], v[192:195], v[48:63]\n"
    "ds_read_b64_tr_b16 v[92:93], v127 offset:8192\n"
    "ds_read_b64_tr_b16 v[94:95], v127 offset:8704\n"
    "v_mfma_f32_16x16x32_bf16 v[104:107], v[116:119], v[80:83], v[104:107]\n"
    "s_waitcnt lgkmcnt(4)\n"
    "v_mfma_f32_32x32x16_bf16 v[48:63], v[84:87], v[196:199], v[48:63]\n"
    "ds_read_b64_tr_b16 v[80:81], v124 offset:16384\n"
    "ds_read_b64_tr_b16 v[82:83], v124 offset:16896\n"
    "s_xor_b32 s60, s73, 0\n"
    "s_mul_i32 s60, s60, 0xe000\n"
    "s_add_u32 m0, s50, 0xe000\n"
    "s_add_u32 s46, s60, 0\n"
    "buffer_load_dwordx4 v249, s[24:27], s46 offen lds\n"
    "s_waitcnt lgkmcnt(4)\n"
    "v_mfma_f32_32x32x16_bf16 v[48:63], v[88:91], v[200:203], v[48:63]\n"
    "ds_read_b64_tr_b16 v[84:85], v124 offset:24576\n"
    "ds_read_b64_tr_b16 v[86:87], v124 offset:25088\n"
    "s_waitcnt lgkmcnt(4)\n"
    "v_mfma_f32_32x32x16_bf16 v[48:63], v[92:95], v[204:207], v[48:63]\n"
    "ds_read_b64_tr_b16 v[88:89], v125 offset:16384\n"
    "ds_read_b64_tr_b16 v[90:91], v125 offset:16896\n"
    "s_mov_b64 s[12:13], s[34:35]\n"
    "s_mov_b32 s14, 0x188000\n"
    "s_mov_b32 s34, 0x1c0000\n"
    "s_mov_b32 s35, 0x20000\n"
    "s_bfe_u32 s46, s72, 0x30000\n"
    "s_add_u32 m0, s50, 0x20000\n"
    "s_mul_i32 s46, s46, 0x38000\n"
    "buffer_load_dwordx4 v78, s[12:15], s46 offen lds\n"
    "s_setprio 2\n"
    "s_waitcnt lgkmcnt(4)\n"
    "v_mfma_f32_32x32x16_bf16 v[48:63], v[80:83], v[208:211], v[48:63]\n"
    "ds_read_b64_tr_b16 v[92:93], v126 offset:16384\n"
    "ds_read_b64_tr_b16 v[94:95], v126 offset:16896\n"
    "v_mfma_f32_16x16x32_bf16 v[104:107], v[116:119], v[80:83], v[104:107]\n"
    "s_waitcnt lgkmcnt(4)\n"
    "v_mfma_f32_32x32x16_bf16 v[48:63], v[84:87], v[224:227], v[48:63]\n"
    "ds_read_b64_tr_b16 v[80:81], v127 offset:16384\n"
    "ds_read_b64_tr_b16 v[82:83], v127 offset:16896\n"
    "s_add_u32 m0, s50, 0x14000\n"
    "s_add_u32 s46, s60, 0x38000\n"
    "buffer_load_dwordx4 v249, s[24:27], s46 offen lds\n"
    "v_mfma_f32_16x16x32_bf16 v[104:107], v[116:119], v[84:87], v[104:107]\n"
    "s_waitcnt lgkmcnt(4)\n"
    "v_mfma_f32_32x32x16_bf16 v[48:63], v[88:91], v[212:215], v[48:63]\n"
    "ds_read_b64_tr_b16 v[84:85], v125 offset:24576\n"
    "ds_read_b64_tr_b16 v[86:87], v125 offset:25088\n"
    "s_waitcnt lgkmcnt(4)\n"
    "v_mfma_f32_32x32x16_bf16 v[48:63], v[92:95], v[216:219], v[48:63]\n"
    "ds_read_b64_tr_b16 v[88:89], v126 offset:24576\n"
    "ds_read_b64_tr_b16 v[90:91], v126 offset:25088\n"
    "v_and_b32 v105, s71, v104\n"
    "v_sub_f32 v104, v104, v105\n"
    "v_lshrrev_b32 v104, 16, v104\n"
    "v_or_b32 v105, v105, v104\n"
    "ds_write_b32 v247, v105 offset:2048\n"
    "s_add_u32 m0, s50, 0x16000\n"
    "s_add_u32 s46, s60, 0x70000\n"
    "buffer_load_dwordx4 v249, s[24:27], s46 offen lds\n"
    "s_setprio 1\n"
    "s_waitcnt lgkmcnt(5)\n"
    "v_mfma_f32_32x32x16_bf16 v[48:63], v[80:83], v[220:223], v[48:63]\n"
    "ds_read_b64_tr_b16 v[92:93], v127 offset:24576\n"
    "ds_read_b64_tr_b16 v[94:95], v127 offset:25088\n"
    "s_waitcnt vmcnt(10) lgkmcnt(0)\n"
    "s_barrier\n"
    "v_mfma_f32_32x32x16_bf16 v[48:63], v[84:87], v[228:231], v[48:63]\n"
    "ds_read_b128 v[108:111], v248 offset:2048\n"
    "v_add_u32 v124, 0xfffec000, v124\n"
    "ds_read_b64_tr_b16 v[80:81], v124\n"
    "ds_read_b64_tr_b16 v[82:83], v124 offset:512\n"
    "v_mfma_f32_32x32x16_bf16 v[48:63], v[88:91], v[232:235], v[48:63]\n"
    "v_add_u32 v125, 0xfffec000, v125\n"
    "ds_read_b64_tr_b16 v[84:85], v125\n"
    "ds_read_b64_tr_b16 v[86:87], v125 offset:512\n"
    "v_mfma_f32_32x32x16_bf16 v[48:63], v[92:95], v[236:239], v[48:63]\n"
    "v_add_u32 v126, 0xfffec000, v126\n"
    "ds_read_b64_tr_b16 v[88:89], v126\n"
    "ds_read_b64_tr_b16 v[90:91], v126 offset:512\n"
    "s_bfe_u32 s46, s72, 0x30004\n"
    "s_add_u32 m0, s50, 0x22000\n"
    "s_mul_i32 s46, s46, 0x38000\n"
    "buffer_load_dwordx4 v78, s[12:15], s46 offen lds\n"
    "s_waitcnt lgkmcnt(6)\n"
    "v_mfma_f32_32x32x16_bf16 v[48:63], v[108:111], v[120:123], v[48:63]\n"
    "s_nop 11\n"
    "v_mul_f32 v48, v100, v48\n"
    "v_mul_f32 v49, v100, v49\n"
    "v_cvt_pk_bf16_f32 v48, v48, v49\n"
    "v_mul_f32 v50, v100, v50\n"
    "v_mul_f32 v51, v100, v51\n"
    "v_cvt_pk_bf16_f32 v49, v50, v51\n"
    "v_mul_f32 v52, v100, v52\n"
    "v_mul_f32 v53, v100, v53\n"
    "v_cvt_pk_bf16_f32 v50, v52, v53\n"
    "v_mul_f32 v54, v100, v54\n"
    "v_mul_f32 v55, v100, v55\n"
    "v_cvt_pk_bf16_f32 v51, v54, v55\n"
    "v_mul_f32 v56, v100, v56\n"
    "v_mul_f32 v57, v100, v57\n"
    "v_cvt_pk_bf16_f32 v52, v56, v57\n"
    "v_mul_f32 v58, v100, v58\n"
    "v_mul_f32 v59, v100, v59\n"
    "v_cvt_pk_bf16_f32 v53, v58, v59\n"
    "v_mul_f32 v60, v100, v60\n"
    "v_mul_f32 v61, v100, v61\n"
    "v_cvt_pk_bf16_f32 v54, v60, v61\n"
    "v_mul_f32 v62, v100, v62\n"
    "v_mul_f32 v63, v100, v63\n"
    "v_cvt_pk_bf16_f32 v55, v62, v63\n"
    "s_and_b32 s56, s56, 0x3ffff\n"
    "v_permlane32_swap_b32 v48, v50\n"
    "v_permlane32_swap_b32 v49, v51\n"
    "v_permlane32_swap_b32 v52, v54\n"
    "v_permlane32_swap_b32 v53, v55\n"
    "s_add_u32 s47, s56, 0x10000\n"
    "buffer_store_dwordx4 v[48:51], v245, s[20:23], s56 offen sc1\n"
    "buffer_store_dwordx4 v[52:55], v245, s[20:23], s47 offen sc1\n"
    "s_branch .Lk_if16_end\n"
    ".Lk_if16:\n"
    "s_bfe_u32 s46, s72, 0x30010\n"
    "s_add_u32 m0, s50, 0x8000\n"
    "s_mul_i32 s46, s46, 0x38000\n"
    "buffer_load_dwordx4 v78, s[8:11], s46 offen lds\n"
    "s_waitcnt lgkmcnt(9)\n"
    "v_add_f32 v108, v108, v110\n"
    "s_waitcnt lgkmcnt(8)\n"
    "v_add_f32 v112, v112, v114\n"
    "v_add_f32 v109, v109, v111\n"
    "v_add_f32 v113, v113, v115\n"
    "v_add_f32 v108, v108, v112\n"
    "v_add_f32 v109, v109, v113\n"
    "v_mul_f32 v108, 0x3b124925, v108\n"
    "v_cmp_gt_u32 vcc, 0x200, v244\n"
    "v_mul_f32 v109, 0x3b124925, v109\n"
    "v_cvt_pk_bf16_f32 v110, -v108, -v108\n"
    "v_fma_f32 v109, -v108, v108, v109\n"
    "v_cndmask_b32 v120, 0, v110, vcc\n"
    "v_mov_b32 v121, v120\n"
    "v_mov_b32 v122, v120\n"
    "v_mov_b32 v123, v120\n"
    "s_waitcnt lgkmcnt(7)\n"
    "s_nop 0\n"
    "v_mfma_f32_32x32x16_bf16 v[32:47], v[24:27], v[120:123], v[32:47]\n"
    "s_cmp_eq_u32 s52, 8\n"
    "s_cselect_b32 s48, 1.0, 0x3e8b97f6\n"
    "v_max_f32 v109, 0, v109\n"
    "v_cvt_pk_bf16_f32 v109, 0, v109\n"
    "v_add_f32 v109, 0x3727c5ac, v109\n"
    "v_cvt_pk_bf16_f32 v109, 0, v109\n"
    "v_sqrt_f32 v109, v109\n"
    "v_add_u32 v240, 1, v240\n"
    "v_cvt_pk_bf16_f32 v109, 0, v109\n"
    "v_rcp_f32 v99, v109\n"
    "s_waitcnt lgkmcnt(2)\n"
    "v_mfma_f32_32x32x16_bf16 v[0:15], v[28:31], v[120:123], v[0:15]\n"
    "v_mul_f32 v98, v109, v109\n"
    "v_mul_f32 v100, s48, v99\n"
    "v_mul_f32 v32, v100, v32\n"
    "v_mul_f32 v33, v100, v33\n"
    "v_cvt_pk_bf16_f32 v32, v32, v33\n"
    "v_mul_f32 v34, v100, v34\n"
    "v_mul_f32 v35, v100, v35\n"
    "v_cvt_pk_bf16_f32 v33, v34, v35\n"
    "v_mul_f32 v36, v100, v36\n"
    "v_mul_f32 v37, v100, v37\n"
    "v_cvt_pk_bf16_f32 v34, v36, v37\n"
    "v_mul_f32 v38, v100, v38\n"
    "v_mul_f32 v39, v100, v39\n"
    "v_cvt_pk_bf16_f32 v35, v38, v39\n"
    "v_mul_f32 v40, v100, v40\n"
    "v_mul_f32 v41, v100, v41\n"
    "v_cvt_pk_bf16_f32 v36, v40, v41\n"
    "v_mul_f32 v42, v100, v42\n"
    "v_mul_f32 v43, v100, v43\n"
    "v_cvt_pk_bf16_f32 v37, v42, v43\n"
    "v_mul_f32 v44, v100, v44\n"
    "v_mul_f32 v45, v100, v45\n"
    "v_cvt_pk_bf16_f32 v38, v44, v45\n"
    "v_mul_f32 v46, v100, v46\n"
    "v_mul_f32 v47, v100, v47\n"
    "v_cvt_pk_bf16_f32 v39, v46, v47\n"
    "v_permlane32_swap_b32 v32, v34\n"
    "v_permlane32_swap_b32 v33, v35\n"
    "v_permlane32_swap_b32 v36, v38\n"
    "v_permlane32_swap_b32 v37, v39\n"
    "s_add_u32 s47, s56, 0x10000\n"
    "buffer_store_dwordx4 v[32:35], v245, s[20:23], s56 offen sc1\n"
    "buffer_store_dwordx4 v[36:39], v245, s[20:23], s47 offen sc1\n"
    "s_add_u32 s56, s56, 0x40000\n"
    "v_mul_f32 v0, v100, v0\n"
    "v_mul_f32 v1, v100, v1\n"
    "v_cvt_pk_bf16_f32 v0, v0, v1\n"
    "v_mul_f32 v2, v100, v2\n"
    "v_mul_f32 v3, v100, v3\n"
    "v_cvt_pk_bf16_f32 v1, v2, v3\n"
    "v_mul_f32 v4, v100, v4\n"
    "v_mul_f32 v5, v100, v5\n"
    "v_cvt_pk_bf16_f32 v2, v4, v5\n"
    "v_mul_f32 v6, v100, v6\n"
    "v_mul_f32 v7, v100, v7\n"
    "v_cvt_pk_bf16_f32 v3, v6, v7\n"
    "v_mul_f32 v8, v100, v8\n"
    "v_mul_f32 v9, v100, v9\n"
    "v_cvt_pk_bf16_f32 v4, v8, v9\n"
    "v_mul_f32 v10, v100, v10\n"
    "v_mul_f32 v11, v100, v11\n"
    "v_cvt_pk_bf16_f32 v5, v10, v11\n"
    "v_mul_f32 v12, v100, v12\n"
    "v_mul_f32 v13, v100, v13\n"
    "v_cvt_pk_bf16_f32 v6, v12, v13\n"
    "v_mul_f32 v14, v100, v14\n"
    "v_mul_f32 v15, v100, v15\n"
    "v_cvt_pk_bf16_f32 v7, v14, v15\n"
    "v_permlane32_swap_b32 v0, v2\n"
    "v_permlane32_swap_b32 v1, v3\n"
    "v_permlane32_swap_b32 v4, v6\n"
    "v_permlane32_swap_b32 v5, v7\n"
    "s_add_u32 s47, s56, 0x10000\n"
    "buffer_store_dwordx4 v[0:3], v245, s[20:23], s56 offen sc1\n"
    "buffer_store_dwordx4 v[4:7], v245, s[20:23], s47 offen sc1\n"
    "v_mul_f32 v96, 0xc0135761, v99\n"
    "v_mul_f32 v108, v99, v99\n"
    "v_mul_f32 v97, 0x3d372713, v96\n"
    "v_mul_f32 v97, v97, v108\n"
    "s_waitcnt vmcnt(9) lgkmcnt(0)\n"
    "s_barrier\n"
    "s_bfe_u32 s46, s72, 0x30014\n"
    "s_add_u32 m0, s50, 0xa000\n"
    "s_mul_i32 s46, s46, 0x38000\n"
    "buffer_load_dwordx4 v78, s[8:11], s46 offen lds\n"
    "s_bfe_u32 s46, s72, 0x30018\n"
    "s_add_u32 m0, s50, 0xc000\n"
    "s_mul_i32 s46, s46, 0x38000\n"
    "buffer_load_dwordx4 v78, s[8:11], s46 offen lds\n"
    "s_xor_b32 s60, s73, 0\n"
    "s_mul_i32 s60, s60, 0xe000\n"
    "s_add_u32 m0, s50, 0xe000\n"
    "s_add_u32 s46, s60, 0\n"
    "buffer_load_dwordx4 v249, s[24:27], s46 offen lds\n"
    "s_mov_b64 s[12:13], s[34:35]\n"
    "s_mov_b32 s14, 0x188000\n"
    "s_mov_b32 s34, 0x1c0000\n"
    "s_mov_b32 s35, 0x20000\n"
    "s_bfe_u32 s46, s72, 0x30000\n"
    "s_add_u32 m0, s50, 0x20000\n"
    "s_mul_i32 s46, s46, 0x38000\n"
    "buffer_load_dwordx4 v78, s[12:15], s46 offen lds\n"
    "s_add_u32 m0, s50, 0x14000\n"
    "s_add_u32 s46, s60, 0x38000\n"
    "buffer_load_dwordx4 v249, s[24:27], s46 offen lds\n"
    "s_add_u32 m0, s50, 0x16000\n"
    "s_add_u32 s46, s60, 0x70000\n"
    "buffer_load_dwordx4 v249, s[24:27], s46 offen lds\n"
    "s_waitcnt vmcnt(10) lgkmcnt(0)\n"
    "s_barrier\n"
    "s_bfe_u32 s46, s72, 0x30004\n"
    "s_add_u32 m0, s50, 0x22000\n"
    "s_mul_i32 s46, s46, 0x38000\n"
    "buffer_load_dwordx4 v78, s[12:15], s46 offen lds\n"
    "v_add_u32 v124, 0xfffec000, v124\n"
    "ds_read_b64_tr_b16 v[80:81], v124\n"
    "ds_read_b64_tr_b16 v[82:83], v124 offset:512\n"
    "v_add_u32 v125, 0xfffec000, v125\n"
    "ds_read_b64_tr_b16 v[84:85], v125\n"
    "ds_read_b64_tr_b16 v[86:87], v125 offset:512\n"
    "v_add_u32 v126, 0xfffec000, v126\n"
    "ds_read_b64_tr_b16 v[88:89], v126\n"
    "ds_read_b64_tr_b16 v[90:91], v126 offset:512\n"
    ".Lk_if16_end:\n"
    "s_setprio 0\n"
    "s_waitcnt lgkmcnt(4)\n"
    "v_mfma_f32_32x32x16_bf16 v[16:31], v[80:83], v[128:131], 0\n"
    "v_add_u32 v127, 0xfffec000, v127\n"
    "ds_read_b64_tr_b16 v[92:93], v127\n"
    "ds_read_b64_tr_b16 v[94:95], v127 offset:512\n"
    "s_mov_b64 s[16:17], s[20:21]\n"
    "s_lshl_b32 s18, s26, 4\n"
    "s_lshr_b32 s30, s26, 2\n"
    "s_mov_b32 s36, 0x77ff77ff\n"
    "s_mov_b32 s37, 0x77ff77ff\n"
    "v_mov_b32 v48, 0x3f803f80\n"
    "v_mov_b32 v49, 0x3f803f80\n"
    "v_mfma_f32_16x16x32_bf16 v[104:107], v[116:119], v[80:83], 0\n"
    "v_mov_b32 v50, 0x3f803f80\n"
    "v_mov_b32 v51, 0x3f803f80\n"
    "s_waitcnt lgkmcnt(4)\n"
    "v_mfma_f32_32x32x16_bf16 v[16:31], v[84:87], v[132:135], v[16:31]\n"
    "ds_read_b64_tr_b16 v[80:81], v124 offset:8192\n"
    "ds_read_b64_tr_b16 v[82:83], v124 offset:8704\n"
    "s_bfe_u32 s46, s72, 0x30008\n"
    "s_add_u32 m0, s50, 0x24000\n"
    "s_mul_i32 s46, s46, 0x38000\n"
    "buffer_load_dwordx4 v78, s[12:15], s46 offen lds\n"
    "v_mov_b32 v52, 0x3f803f80\n"
    "v_mov_b32 v53, 0x3f803f80\n"
    "v_mov_b32 v54, 0x3f803f80\n"
    "v_mov_b32 v55, 0x3f803f80\n"
    "v_mov_b32 v56, 0x3f803f80\n"
    "s_waitcnt lgkmcnt(4)\n"
    "v_mfma_f32_32x32x16_bf16 v[16:31], v[88:91], v[136:139], v[16:31]\n"
    "ds_read_b64_tr_b16 v[84:85], v125 offset:8192\n"
    "ds_read_b64_tr_b16 v[86:87], v125 offset:8704\n"
    "v_mov_b32 v57, 0x3f803f80\n"
    "v_mov_b32 v58, 0x3f803f80\n"
    "v_mov_b32 v59, 0x3f803f80\n"
    "v_mov_b32 v60, 0x3f803f80\n"
    "v_mov_b32 v61, 0x3f803f80\n"
    "s_waitcnt lgkmcnt(4)\n"
    "v_mfma_f32_32x32x16_bf16 v[16:31], v[92:95], v[140:143], v[16:31]\n"
    "ds_read_b64_tr_b16 v[88:89], v126 offset:8192\n"
    "ds_read_b64_tr_b16 v[90:91], v126 offset:8704\n"
    "s_bfe_u32 s46, s72, 0x3000c\n"
    "s_add_u32 m0, s50, 0x18000\n"
    "s_mul_i32 s46, s46, 0x38000\n"
    "buffer_load_dwordx4 v78, s[12:15], s46 offen lds\n"
    "v_mov_b32 v62, 0x3f803f80\n"
    "v_mov_b32 v63, 0x3f803f80\n"
    "s_setprio 3\n"
    "s_waitcnt lgkmcnt(4)\n"
    "v_mfma_f32_32x32x16_bf16 v[16:31], v[80:83], v[144:147], v[16:31]\n"
    "ds_read_b64_tr_b16 v[92:93], v127 offset:8192\n"
    "ds_read_b64_tr_b16 v[94:95], v127 offset:8704\n"
    "v_mfma_f32_16x16x32_bf16 v[104:107], v[116:119], v[80:83], v[104:107]\n"
    "s_waitcnt lgkmcnt(4)\n"
    "v_mfma_f32_32x32x16_bf16 v[16:31], v[84:87], v[148:151], v[16:31]\n"
    "ds_read_b64_tr_b16 v[80:81], v124 offset:49152\n"
    "ds_read_b64_tr_b16 v[82:83], v124 offset:49664\n"
    "s_bfe_u32 s46, s72, 0x30010\n"
    "s_add_u32 m0, s50, 0x1a000\n"
    "s_mul_i32 s46, s46, 0x38000\n"
    "buffer_load_dwordx4 v78, s[12:15], s46 offen lds\n"
    "s_waitcnt vmcnt(3)\n"
    "s_barrier\n"
    "s_waitcnt lgkmcnt(4)\n"
    "v_mfma_f32_32x32x16_bf16 v[16:31], v[88:91], v[152:155], v[16:31]\n"
    "s_cmp_eq_u32 s40, 0\n"
    "s_cbranch_scc0 .Lk_if17\n"
    "s_mov_b64 exec, 0xff\n"
    "buffer_store_dwordx4 v[240:243], v244, s[20:23], s58 offen sc1\n"
    "s_mov_b64 exec, -1\n"
    ".Lk_if17:\n"
    "ds_read_b64_tr_b16 v[84:85], v125 offset:49152\n"
    "ds_read_b64_tr_b16 v[86:87], v125 offset:49664\n"
    "s_waitcnt lgkmcnt(4)\n"
    "v_mfma_f32_32x32x16_bf16 v[16:31], v[92:95], v[156:159], v[16:31]\n"
    "ds_read_b64_tr_b16 v[88:89], v126 offset:49152\n"
    "ds_read_b64_tr_b16 v[90:91], v126 offset:49664\n"
    "s_bfe_u32 s46, s72, 0x30014\n"
    "s_add_u32 m0, s50, 0x1c000\n"
    "s_mul_i32 s46, s46, 0x38000\n"
    "buffer_load_dwordx4 v78, s[12:15], s46 offen lds\n"
    "s_setprio 2\n"
    "s_waitcnt lgkmcnt(4)\n"
    "v_mfma_f32_32x32x16_bf16 v[16:31], v[80:83], v[160:163], v[16:31]\n"
    "ds_read_b64_tr_b16 v[92:93], v127 offset:49152\n"
    "ds_read_b64_tr_b16 v[94:95], v127 offset:49664\n"
    "v_mfma_f32_16x16x32_bf16 v[104:107], v[116:119], v[80:83], v[104:107]\n"
    "s_waitcnt lgkmcnt(4)\n"
    "v_mfma_f32_32x32x16_bf16 v[16:31], v[84:87], v[164:167], v[16:31]\n"
    "ds_read_b64_tr_b16 v[80:81], v124 offset:57344\n"
    "ds_read_b64_tr_b16 v[82:83], v124 offset:57856\n"
    "s_bfe_u32 s46, s72, 0x30018\n"
    "s_add_u32 m0, s50, 0x1e000\n"
    "s_mul_i32 s46, s46, 0x38000\n"
    "buffer_load_dwordx4 v78, s[12:15], s46 offen lds\n"
    "s_waitcnt lgkmcnt(4)\n"
    "v_mfma_f32_32x32x16_bf16 v[16:31], v[88:91], v[168:171], v[16:31]\n"
    "ds_read_b64_tr_b16 v[84:85], v125 offset:57344\n"
    "ds_read_b64_tr_b16 v[86:87], v125 offset:57856\n"
    "s_waitcnt lgkmcnt(4)\n"
    "v_mfma_f32_32x32x16_bf16 v[16:31], v[92:95], v[172:175], v[16:31]\n"
    "ds_read_b64_tr_b16 v[88:89], v126 offset:57344\n"
    "ds_read_b64_tr_b16 v[90:91], v126 offset:57856\n"
    "s_xor_b32 s60, s73, 1\n"
    "s_mul_i32 s60, s60, 0xe000\n"
    "s_add_u32 m0, s50, 0\n"
    "s_add_u32 s46, s60, 0\n"
    "buffer_load_dwordx4 v249, s[24:27], s46 offen lds\n"
    "s_setprio 1\n"
    "s_waitcnt lgkmcnt(4)\n"
    "v_mfma_f32_32x32x16_bf16 v[16:31], v[80:83], v[176:179], v[16:31]\n"
    "ds_read_b64_tr_b16 v[92:93], v127 offset:57344\n"
    "ds_read_b64_tr_b16 v[94:95], v127 offset:57856\n"
    "v_mfma_f32_16x16x32_bf16 v[104:107], v[116:119], v[80:83], v[104:107]\n"
    "s_waitcnt lgkmcnt(4)\n"
    "v_mfma_f32_32x32x16_bf16 v[16:31], v[84:87], v[180:183], v[16:31]\n"
    "ds_read_b64_tr_b16 v[80:81], v124 offset:16384\n"
    "ds_read_b64_tr_b16 v[82:83], v124 offset:16896\n"
    "s_add_u32 m0, s50, 0x2000\n"
    "s_add_u32 s46, s60, 0x38000\n"
    "buffer_load_dwordx4 v249, s[24:27], s46 offen lds\n"
    "s_barrier\n"
    "s_waitcnt lgkmcnt(4)\n"
    "v_mfma_f32_32x32x16_bf16 v[16:31], v[88:91], v[184:187], v[16:31]\n"
    "ds_read_b64_tr_b16 v[84:85], v125 offset:16384\n"
    "ds_read_b64_tr_b16 v[86:87], v125 offset:16896\n"
    "s_waitcnt lgkmcnt(4)\n"
    "v_mfma_f32_32x32x16_bf16 v[16:31], v[92:95], v[188:191], v[16:31]\n"
    "ds_read_b64_tr_b16 v[88:89], v126 offset:16384\n"
    "ds_read_b64_tr_b16 v[90:91], v126 offset:16896\n"
    "s_add_u32 m0, s50, 0x4000\n"
    "s_add_u32 s46, s60, 0x70000\n"
    "buffer_load_dwordx4 v249, s[24:27], s46 offen lds\n"
    "s_setprio 0\n"
    "s_waitcnt lgkmcnt(4)\n"
    "v_mfma_f32_32x32x16_bf16 v[16:31], v[80:83], v[192:195], v[16:31]\n"
    "ds_read_b64_tr_b16 v[92:93], v127 offset:16384\n"
    "ds_read_b64_tr_b16 v[94:95], v127 offset:16896\n"
    "v_mfma_f32_16x16x32_bf16 v[104:107], v[116:119], v[80:83], v[104:107]\n"
    "s_waitcnt lgkmcnt(4)\n"
    "v_mfma_f32_32x32x16_bf16 v[16:31], v[84:87], v[196:199], v[16:31]\n"
    "ds_read_b64_tr_b16 v[80:81], v124 offset:24576\n"
    "ds_read_b64_tr_b16 v[82:83], v124 offset:25088\n"
    "s_xor_b32 s60, s73, 2\n"
    "s_mul_i32 s60, s60, 0xe000\n"
    "s_add_u32 m0, s50, 0x6000\n"
    "s_add_u32 s46, s60, 0\n"
    "buffer_load_dwordx4 v249, s[24:27], s46 offen lds\n"
    "s_waitcnt lgkmcnt(4)\n"
    "v_mfma_f32_32x32x16_bf16 v[16:31], v[88:91], v[200:203], v[16:31]\n"
    "ds_read_b64_tr_b16 v[84:85], v124 offset:32768\n"
    "ds_read_b64_tr_b16 v[86:87], v124 offset:33280\n"
    "s_waitcnt lgkmcnt(4)\n"
    "v_mfma_f32_32x32x16_bf16 v[16:31], v[92:95], v[204:207], v[16:31]\n"
    "ds_read_b64_tr_b16 v[88:89], v125 offset:24576\n"
    "ds_read_b64_tr_b16 v[90:91], v125 offset:25088\n"
    "s_add_u32 m0, s50, 0x10000\n"
    "s_add_u32 s46, s60, 0x38000\n"
    "buffer_load_dwordx4 v249, s[24:27], s46 offen lds\n"
    "s_setprio 3\n"
    "s_waitcnt lgkmcnt(4)\n"
    "v_mfma_f32_32x32x16_bf16 v[16:31], v[80:83], v[208:211], v[16:31]\n"
    "ds_read_b64_tr_b16 v[92:93], v126 offset:24576\n"
    "ds_read_b64_tr_b16 v[94:95], v126 offset:25088\n"
    "v_mfma_f32_16x16x32_bf16 v[104:107], v[116:119], v[80:83], v[104:107]\n"
    "s_waitcnt lgkmcnt(4)\n"
    "v_mfma_f32_32x32x16_bf16 v[16:31], v[84:87], v[224:227], v[16:31]\n"
    "ds_read_b64_tr_b16 v[80:81], v127 offset:24576\n"
    "ds_read_b64_tr_b16 v[82:83], v127 offset:25088\n"
    "v_mfma_f32_16x16x32_bf16 v[104:107], v[116:119], v[84:87], v[104:107]\n"
    "s_waitcnt lgkmcnt(4)\n"
    "v_mfma_f32_32x32x16_bf16 v[16:31], v[88:91], v[212:215], v[16:31]\n"
    "ds_read_b64_tr_b16 v[84:85], v125 offset:32768\n"
    "ds_read_b64_tr_b16 v[86:87], v125 offset:33280\n"
    "s_waitcnt lgkmcnt(4)\n"
    "v_mfma_f32_32x32x16_bf16 v[16:31], v[92:95], v[216:219], v[16:31]\n"
    "ds_read_b64_tr_b16 v[88:89], v126 offset:32768\n"
    "ds_read_b64_tr_b16 v[90:91], v126 offset:33280\n"
    "v_and_b32 v105, s71, v104\n"
    "v_sub_f32 v104, v104, v105\n"
    "v_lshrrev_b32 v104, 16, v104\n"
    "v_or_b32 v105, v105, v104\n"
    "ds_write_b32 v247, v105 offset:3072\n"
    "s_setprio 2\n"
    "s_waitcnt lgkmcnt(5)\n"
    "v_mfma_f32_32x32x16_bf16 v[16:31], v[80:83], v[220:223], v[16:31]\n"
    "ds_read_b64_tr_b16 v[92:93], v127 offset:32768\n"
    "ds_read_b64_tr_b16 v[94:95], v127 offset:33280\n"
    "s_waitcnt vmcnt(5) lgkmcnt(0)\n"
    "s_barrier\n"
    "v_mfma_f32_32x32x16_bf16 v[16:31], v[84:87], v[228:231], v[16:31]\n"
    "ds_read_b128 v[108:111], v248 offset:3072\n"
    "v_add_u32 v124, 0x14000, v124\n"
    "ds_read_b64_tr_b16 v[80:81], v124 offset:32768\n"
    "ds_read_b64_tr_b16 v[82:83], v124 offset:33280\n"
    "v_mfma_f32_32x32x16_bf16 v[16:31], v[88:91], v[232:235], v[16:31]\n"
    "v_add_u32 v125, 0x14000, v125\n"
    "ds_read_b64_tr_b16 v[84:85], v125 offset:32768\n"
    "ds_read_b64_tr_b16 v[86:87], v125 offset:33280\n"
    "v_mfma_f32_32x32x16_bf16 v[16:31], v[92:95], v[236:239], v[16:31]\n"
    "v_add_u32 v126, 0x14000, v126\n"
    "ds_read_b64_tr_b16 v[88:89], v126 offset:32768\n"
    "ds_read_b64_tr_b16 v[90:91], v126 offset:33280\n"
    "s_waitcnt lgkmcnt(6)\n"
    "v_mfma_f32_32x32x16_bf16 v[16:31], v[108:111], v[120:123], v[16:31]\n"
    "s_setprio 1\n"
    "s_waitcnt lgkmcnt(4)\n"
    "v_mfma_f32_32x32x16_bf16 v[64:79], v[80:83], v[128:131], 0\n"
    "v_add_u32 v127, 0x14000, v127\n"
    "ds_read_b64_tr_b16 v[92:93], v127 offset:32768\n"
    "ds_read_b64_tr_b16 v[94:95], v127 offset:33280\n"
    "s_mov_b64 exec, 1\n"
    "buffer_store_dwordx2 v[240:241], off, s[20:23], s59 sc1\n"
    "s_mov_b64 exec, -1\n"
    "v_mfma_f32_16x16x32_bf16 v[104:107], v[116:119], v[80:83], 0\n"
    "s_waitcnt lgkmcnt(4)\n"
    "v_mfma_f32_32x32x16_bf16 v[64:79], v[84:87], v[132:135], v[64:79]\n"
    "ds_read_b64_tr_b16 v[80:81], v124 offset:40960\n"
    "ds_read_b64_tr_b16 v[82:83], v124 offset:41472\n"
    "s_add_u32 m0, s50, 0x12000\n"
    "s_add_u32 s46, s60, 0x70000\n"
    "buffer_load_dwordx4 v249, s[24:27], s46 offen lds\n"
    "s_waitcnt lgkmcnt(4)\n"
    "v_mfma_f32_32x32x16_bf16 v[64:79], v[88:91], v[136:139], v[64:79]\n"
    "ds_read_b64_tr_b16 v[84:85], v125 offset:40960\n"
    "ds_read_b64_tr_b16 v[86:87], v125 offset:41472\n"
    "v_mul_f32 v100, v16, v16\n"
    "v_mul_f32 v101, v17, v17\n"
    "v_mul_f32 v102, v18, v18\n"
    "v_mul_f32 v103, v19, v19\n"
    "v_fma_f32 v100, v100, v97, v96\n"
    "s_waitcnt lgkmcnt(4)\n"
    "v_mfma_f32_32x32x16_bf16 v[64:79], v[92:95], v[140:143], v[64:79]\n"
    "ds_read_b64_tr_b16 v[88:89], v126 offset:40960\n"
    "ds_read_b64_tr_b16 v[90:91], v126 offset:41472\n"
    "v_fma_f32 v101, v101, v97, v96\n"
    "v_fma_f32 v102, v102, v97, v96\n"
    "v_fma_f32 v103, v103, v97, v96\n"
    "v_mul_f32 v100, v16, v100\n"
    "v_mul_f32 v101, v17, v101\n"
    "s_setprio 0\n"
    "s_waitcnt lgkmcnt(4)\n"
    "v_mfma_f32_32x32x16_bf16 v[64:79], v[80:83], v[144:147], v[64:79]\n"
    "ds_read_b64_tr_b16 v[92:93], v127 offset:40960\n"
    "ds_read_b64_tr_b16 v[94:95], v127 offset:41472\n"
    "v_mul_f32 v102, v18, v102\n"
    "v_mul_f32 v103, v19, v103\n"
    "v_exp_f32 v100, v100\n"
    "v_exp_f32 v101, v101\n"
    "v_mfma_f32_16x16x32_bf16 v[104:107], v[116:119], v[80:83], v[104:107]\n"
    "v_exp_f32 v102, v102\n"
    "s_waitcnt lgkmcnt(4)\n"
    "v_mfma_f32_32x32x16_bf16 v[64:79], v[84:87], v[148:151], v[64:79]\n"
    "ds_read_b64_tr_b16 v[80:81], v124 offset:49152\n"
    "ds_read_b64_tr_b16 v[82:83], v124 offset:49664\n"
    "v_exp_f32 v103, v103\n"
    "v_fma_f32 v100, v100, v98, v98\n"
    "v_fma_f32 v101, v101, v98, v98\n"
    "s_waitcnt lgkmcnt(4)\n"
    "v_mfma_f32_32x32x16_bf16 v[64:79], v[88:91], v[152:155], v[64:79]\n"
    "ds_read_b64_tr_b16 v[84:85], v125 offset:49152\n"
    "ds_read_b64_tr_b16 v[86:87], v125 offset:49664\n"
    "s_mov_b32 s46, 0xf00000\n"
    "s_mov_b64 exec, 0xffff\n"
    "buffer_load_dwordx2 v[242:243], v250, s[20:23], s46 offen sc1\n"
    "s_mov_b64 exec, -1\n"
    "v_fma_f32 v102, v102, v98, v98\n"
    "v_fma_f32 v103, v103, v98, v98\n"
    "v_rcp_f32 v100, v100\n"
    "v_rcp_f32 v101, v101\n"
    "s_waitcnt lgkmcnt(4)\n"
    "v_mfma_f32_32x32x16_bf16 v[64:79], v[92:95], v[156:159], v[64:79]\n"
    "ds_read_b64_tr_b16 v[88:89], v126 offset:49152\n"
    "ds_read_b64_tr_b16 v[90:91], v126 offset:49664\n"
    "v_rcp_f32 v102, v102\n"
    "v_rcp_f32 v103, v103\n"
    "s_setprio 3\n"
    "s_waitcnt lgkmcnt(4)\n"
    "v_mfma_f32_32x32x16_bf16 v[64:79], v[80:83], v[160:163], v[64:79]\n"
    "ds_read_b64_tr_b16 v[92:93], v127 offset:49152\n"
    "ds_read_b64_tr_b16 v[94:95], v127 offset:49664\n"
    "v_mul_f32 v16, v16, v100\n"
    "v_mul_f32 v17, v17, v101\n"
    "v_mul_f32 v18, v18, v102\n"
    "v_mul_f32 v19, v19, v103\n"
    "v_mul_f32 v100, v20, v20\n"
    "v_mfma_f32_16x16x32_bf16 v[104:107], v[116:119], v[80:83], v[104:107]\n"
    "v_mul_f32 v101, v21, v21\n"
    "v_mul_f32 v102, v22, v22\n"
    "s_waitcnt lgkmcnt(4)\n"
    "v_mfma_f32_32x32x16_bf16 v[64:79], v[84:87], v[164:167], v[64:79]\n"
    "ds_read_b64_tr_b16 v[80:81], v124\n"
    "ds_read_b64_tr_b16 v[82:83], v124 offset:512\n"
    "v_mul_f32 v103, v23, v23\n"
    "v_fma_f32 v100, v100, v97, v96\n"
    "v_fma_f32 v101, v101, v97, v96\n"
    "v_fma_f32 v102, v102, v97, v96\n"
    "v_fma_f32 v103, v103, v97, v96\n"
    "s_waitcnt lgkmcnt(4)\n"
    "v_mfma_f32_32x32x16_bf16 v[64:79], v[88:91], v[168:171], v[64:79]\n"
    "ds_read_b64_tr_b16 v[84:85], v125\n"
    "ds_read_b64_tr_b16 v[86:87], v125 offset:512\n"
    "v_mul_f32 v100, v20, v100\n"
    "v_mul_f32 v101, v21, v101\n"
    "v_mul_f32 v102, v22, v102\n"
    "v_mul_f32 v103, v23, v103\n"
    "v_exp_f32 v100, v100\n"
    "s_waitcnt lgkmcnt(4)\n"
    "v_mfma_f32_32x32x16_bf16 v[64:79], v[92:95], v[172:175], v[64:79]\n"
    "ds_read_b64_tr_b16 v[88:89], v126\n"
    "ds_read_b64_tr_b16 v[90:91], v126 offset:512\n"
    "v_exp_f32 v101, v101\n"
    "v_exp_f32 v102, v102\n"
    "s_setprio 2\n"
    "s_waitcnt lgkmcnt(4)\n"
    "v_mfma_f32_32x32x16_bf16 v[64:79], v[80:83], v[176:179], v[64:79]\n"
    "ds_read_b64_tr_b16 v[92:93], v127\n"
    "ds_read_b64_tr_b16 v[94:95], v127 offset:512\n"
    "v_exp_f32 v103, v103\n"
    "v_fma_f32 v100, v100, v98, v98\n"
    "v_fma_f32 v101, v101, v98, v98\n"
    "v_fma_f32 v102, v102, v98, v98\n"
    "v_mfma_f32_16x16x32_bf16 v[104:107], v[116:119], v[80:83], v[104:107]\n"
    "v_fma_f32 v103, v103, v98, v98\n"
    "v_rcp_f32 v100, v100\n"
    "s_waitcnt lgkmcnt(4)\n"
    "v_mfma_f32_32x32x16_bf16 v[64:79], v[84:87], v[180:183], v[64:79]\n"
    "ds_read_b64_tr_b16 v[80:81], v124 offset:8192\n"
    "ds_read_b64_tr_b16 v[82:83], v124 offset:8704\n"
    "v_rcp_f32 v101, v101\n"
    "v_rcp_f32 v102, v102\n"
    "s_waitcnt lgkmcnt(4)\n"
    "v_mfma_f32_32x32x16_bf16 v[64:79], v[88:91], v[184:187], v[64:79]\n"
    "ds_read_b64_tr_b16 v[84:85], v125 offset:8192\n"
    "ds_read_b64_tr_b16 v[86:87], v125 offset:8704\n"
    "v_rcp_f32 v103, v103\n"
    "v_mul_f32 v20, v20, v100\n"
    "v_mul_f32 v21, v21, v101\n"
    "v_mul_f32 v22, v22, v102\n"
    "s_waitcnt lgkmcnt(4)\n"
    "v_mfma_f32_32x32x16_bf16 v[64:79], v[92:95], v[188:191], v[64:79]\n"
    "ds_read_b64_tr_b16 v[88:89], v126 offset:8192\n"
    "ds_read_b64_tr_b16 v[90:91], v126 offset:8704\n"
    "v_mul_f32 v23, v23, v103\n"
    "v_mul_f32 v100, v24, v24\n"
    "v_mul_f32 v101, v25, v25\n"
    "v_mul_f32 v102, v26, v26\n"
    "v_mul_f32 v103, v27, v27\n"
    "s_setprio 1\n"
    "s_waitcnt lgkmcnt(4)\n"
    "v_mfma_f32_32x32x16_bf16 v[64:79], v[80:83], v[192:195], v[64:79]\n"
    "ds_read_b64_tr_b16 v[92:93], v127 offset:8192\n"
    "ds_read_b64_tr_b16 v[94:95], v127 offset:8704\n"
    "v_fma_f32 v100, v100, v97, v96\n"
    "v_fma_f32 v101, v101, v97, v96\n"
    "v_fma_f32 v102, v102, v97, v96\n"
    "v_fma_f32 v103, v103, v97, v96\n"
    "v_mul_f32 v100, v24, v100\n"
    "v_mfma_f32_16x16x32_bf16 v[104:107], v[116:119], v[80:83], v[104:107]\n"
    "v_mul_f32 v101, v25, v101\n"
    "v_mul_f32 v102, v26, v102\n"
    "s_waitcnt lgkmcnt(4)\n"
    "v_mfma_f32_32x32x16_bf16 v[64:79], v[84:87], v[196:199], v[64:79]\n"
    "s_waitcnt vmcnt(0)\n"
    "v_cmp_ne_u64 vcc, v[242:243], v[240:241]\n"
    "s_nop 1\n"
    "s_and_b32 s45, vcc_lo, 0xffff\n"
    "s_cselect_b32 s52, 1, 0\n"
    "ds_read_b64_tr_b16 v[80:81], v124 offset:16384\n"
    "ds_read_b64_tr_b16 v[82:83], v124 offset:16896\n"
    "buffer_load_dwordx4 v[0:3], v245, s[20:23], s65 offen sc1\n"
    "v_mul_f32 v103, v27, v103\n"
    "v_exp_f32 v100, v100\n"
    "v_exp_f32 v101, v101\n"
    "s_waitcnt lgkmcnt(4)\n"
    "v_mfma_f32_32x32x16_bf16 v[64:79], v[88:91], v[200:203], v[64:79]\n"
    "ds_read_b64_tr_b16 v[84:85], v124 offset:24576\n"
    "ds_read_b64_tr_b16 v[86:87], v124 offset:25088\n"
    "buffer_load_dwordx4 v[32:35], v245, s[20:23], s61 offen sc1\n"
    "v_exp_f32 v102, v102\n"
    "v_exp_f32 v103, v103\n"
    "v_fma_f32 v100, v100, v98, v98\n"
    "s_waitcnt lgkmcnt(4)\n"
    "v_mfma_f32_32x32x16_bf16 v[64:79], v[92:95], v[204:207], v[64:79]\n"
    "ds_read_b64_tr_b16 v[88:89], v125 offset:16384\n"
    "ds_read_b64_tr_b16 v[90:91], v125 offset:16896\n"
    "buffer_load_dwordx4 v[36:39], v245, s[20:23], s62 offen sc1\n"
    "v_fma_f32 v101, v101, v98, v98\n"
    "v_fma_f32 v102, v102, v98, v98\n"
    "v_fma_f32 v103, v103, v98, v98\n"
    "v_rcp_f32 v100, v100\n"
    "s_setprio 0\n"
    "s_waitcnt lgkmcnt(4)\n"
    "v_mfma_f32_32x32x16_bf16 v[64:79], v[80:83], v[208:211], v[64:79]\n"
    "ds_read_b64_tr_b16 v[92:93], v126 offset:16384\n"
    "ds_read_b64_tr_b16 v[94:95], v126 offset:16896\n"
    "s_xor_b32 s47, s63, 0\n"
    "s_mov_b64 exec, s[36:37]\n"
    "buffer_load_dwordx4 v[48:51], v254, s[20:23], s47 offen sc1\n"
    "buffer_load_dwordx4 v[52:55], v254, s[20:23], s47 offen offset:2048 sc1\n"
    "s_mov_b64 exec, -1\n"
    "v_rcp_f32 v101, v101\n"
    "v_rcp_f32 v102, v102\n"
    "v_rcp_f32 v103, v103\n"
    "v_mfma_f32_16x16x32_bf16 v[104:107], v[116:119], v[80:83], v[104:107]\n"
    "v_mul_f32 v24, v24, v100\n"
    "s_waitcnt lgkmcnt(4)\n"
    "v_mfma_f32_32x32x16_bf16 v[64:79], v[84:87], v[224:227], v[64:79]\n"
    "ds_read_b64_tr_b16 v[80:81], v127 offset:16384\n"
    "ds_read_b64_tr_b16 v[82:83], v127 offset:16896\n"
    "v_mul_f32 v25, v25, v101\n"
    "v_mul_f32 v26, v26, v102\n"
    "v_mul_f32 v27, v27, v103\n"
    "v_mul_f32 v100, v28, v28\n"
    "v_mul_f32 v101, v29, v29\n"
    "v_mfma_f32_16x16x32_bf16 v[104:107], v[116:119], v[84:87], v[104:107]\n"
    "v_mul_f32 v102, v30, v30\n"
    "v_mul_f32 v103, v31, v31\n"
    "s_waitcnt lgkmcnt(4)\n"
    "v_mfma_f32_32x32x16_bf16 v[64:79], v[88:91], v[212:215], v[64:79]\n"
    "ds_read_b64_tr_b16 v[84:85], v125 offset:24576\n"
    "ds_read_b64_tr_b16 v[86:87], v125 offset:25088\n"
    "s_xor_b32 s47, s61, 0x200\n"
    "buffer_load_dwordx4 v[40:43], v245, s[20:23], s47 offen sc1\n"
    "v_fma_f32 v100, v100, v97, v96\n"
    "v_fma_f32 v101, v101, v97, v96\n"
    "v_fma_f32 v102, v102, v97, v96\n"
    "v_fma_f32 v103, v103, v97, v96\n"
    "v_mul_f32 v100, v28, v100\n"
    "s_waitcnt lgkmcnt(4)\n"
    "v_mfma_f32_32x32x16_bf16 v[64:79], v[92:95], v[216:219], v[64:79]\n"
    "ds_read_b64_tr_b16 v[88:89], v126 offset:24576\n"
    "ds_read_b64_tr_b16 v[90:91], v126 offset:25088\n"
    "v_and_b32 v105, s71, v104\n"
    "v_sub_f32 v104, v104, v105\n"
    "v_lshrrev_b32 v104, 16, v104\n"
    "v_or_b32 v105, v105, v104\n"
    "ds_write_b32 v247, v105\n"
    "s_xor_b32 s47, s62, 0x200\n"
    "buffer_load_dwordx4 v[44:47], v245, s[20:23], s47 offen sc1\n"
    "s_setprio 3\n"
    "s_waitcnt lgkmcnt(5)\n"
    "v_mfma_f32_32x32x16_bf16 v[64:79], v[80:83], v[220:223], v[64:79]\n"
    "ds_read_b64_tr_b16 v[92:93], v127 offset:24576\n"
    "ds_read_b64_tr_b16 v[94:95], v127 offset:25088\n"
    "s_xor_b32 s47, s63, 0x1000\n"
    "s_mov_b64 exec, s[36:37]\n"
    "buffer_load_dwordx4 v[56:59], v254, s[20:23], s47 offen sc1\n"
    "buffer_load_dwordx4 v[60:63], v254, s[20:23], s47 offen offset:2048 sc1\n"
    "s_mov_b64 exec, -1\n"
    "v_mul_f32 v101, v29, v101\n"
    "v_mul_f32 v102, v30, v102\n"
    "v_mul_f32 v103, v31, v103\n"
    "v_exp_f32 v100, v100\n"
    "s_cmp_eq_u32 s52, 1\n"
    "s_cbranch_scc0 .Lk_if19\n"
    "s_mov_b32 s70, 0x40000\n"
    ".Lk_lp18:\n"
    "s_sleep 1\n"
    "s_mov_b32 s46, 0xf00000\n"
    "s_mov_b64 exec, 0xffff\n"
    "buffer_load_dwordx2 v[242:243], v250, s[20:23], s46 offen sc1\n"
    "s_mov_b64 exec, -1\n"
    "s_waitcnt vmcnt(0)\n"
    "v_cmp_ne_u64 vcc, v[242:243], v[240:241]\n"
    "s_sub_u32 s70, s70, 1\n"
    "s_nop 0\n"
    "s_cselect_b64 s[38:39], 0, vcc\n"
    "s_and_b32 s45, s38, 0xffff\n"
    "s_cbranch_scc1 .Lk_lp18\n"
    "buffer_load_dwordx4 v[0:3], v245, s[20:23], s65 offen sc1\n"
    "buffer_load_dwordx4 v[32:35], v245, s[20:23], s61 offen sc1\n"
    "buffer_load_dwordx4 v[36:39], v245, s[20:23], s62 offen sc1\n"
    "s_xor_b32 s47, s63, 0\n"
    "s_mov_b64 exec, s[36:37]\n"
    "buffer_load_dwordx4 v[48:51], v254, s[20:23], s47 offen sc1\n"
    "buffer_load_dwordx4 v[52:55], v254, s[20:23], s47 offen offset:2048 sc1\n"
    "s_mov_b64 exec, -1\n"
    "s_xor_b32 s47, s61, 0x200\n"
    "buffer_load_dwordx4 v[40:43], v245, s[20:23], s47 offen sc1\n"
    "s_xor_b32 s47, s62, 0x200\n"
    "buffer_load_dwordx4 v[44:47], v245, s[20:23], s47 offen sc1\n"
    "s_xor_b32 s47, s63, 0x1000\n"
    "s_mov_b64 exec, s[36:37]\n"
    "buffer_load_dwordx4 v[56:59], v254, s[20:23], s47 offen sc1\n"
    "buffer_load_dwordx4 v[60:63], v254, s[20:23], s47 offen offset:2048 sc1\n"
    "s_mov_b64 exec, -1\n"
    "s_waitcnt vmcnt(0)\n"
    ".Lk_if19:\n"
    "s_lshr_b32 s45, s40, 2\n"
    "s_xor_b32 s45, s45, s41\n"
    "s_add_u32 s47, s50, 0xa000\n"
    "v_add_u32 v246, s47, v244\n"
    "s_bitcmp1_b32 s45, 0\n"
    "s_cbranch_scc0 .Lk_if21\n"
    "s_bitcmp1_b32 s41, 0\n"
    "s_cbranch_scc0 .Lk_if20\n"
    "s_mov_b32 exec_hi, 0\n"
    "s_waitcnt vmcnt(8)\n"
    "v_mov_b32 v0, 0\n"
    "v_mov_b32 v1, 0\n"
    "s_mov_b64 exec, -1\n"
    "s_branch .Lk_if20_end\n"
    ".Lk_if20:\n"
    "s_mov_b32 exec_lo, 0\n"
    "s_waitcnt vmcnt(8)\n"
    "v_mov_b32 v2, 0\n"
    "v_mov_b32 v3, 0\n"
    "s_mov_b64 exec, -1\n"
    ".Lk_if20_end:\n"
    ".Lk_if21:\n"
    "s_waitcnt vmcnt(8)\n"
    "ds_write_b128 v246, v[0:3]\n"
    "v_and_b32 v246, 0xffffefff, v246\n"
    "s_waitcnt lgkmcnt(0)\n"
    "s_barrier\n"
    "v_mfma_f32_32x32x16_bf16 v[64:79], v[84:87], v[228:231], v[64:79]\n"
    "ds_read_b128 v[108:111], v248\n"
    "v_xor_b32 v250, 0x400, v246\n"
    "ds_read_b128 v[112:115], v250\n"
    "ds_read_b128 v[116:119], v250 offset:4096\n"
    "v_exp_f32 v101, v101\n"
    "v_exp_f32 v102, v102\n"
    "v_exp_f32 v103, v103\n"
    "v_mfma_f32_32x32x16_bf16 v[64:79], v[88:91], v[232:235], v[64:79]\n"
    "v_fma_f32 v100, v100, v98, v98\n"
    "v_fma_f32 v101, v101, v98, v98\n"
    "v_fma_f32 v102, v102, v98, v98\n"
    "v_fma_f32 v103, v103, v98, v98\n"
    "v_mfma_f32_32x32x16_bf16 v[64:79], v[92:95], v[236:239], v[64:79]\n"
    "v_rcp_f32 v100, v100\n"
    "v_rcp_f32 v101, v101\n"
    "v_rcp_f32 v102, v102\n"
    "s_waitcnt lgkmcnt(2)\n"
    "v_mfma_f32_32x32x16_bf16 v[64:79], v[108:111], v[120:123], v[64:79]\n"
    "v_rcp_f32 v103, v103\n"
    "v_mul_f32 v28, v28, v100\n"
    "v_mul_f32 v29, v29, v101\n"
    "v_mul_f32 v30, v30, v102\n"
    "v_mul_f32 v31, v31, v103\n"
    "s_waitcnt vmcnt(7) lgkmcnt(1)\n"
    "v_mfma_f32_32x32x16_bf16 v[0:15], v[32:35], v[112:115], 0\n"
    "v_xor_b32 v250, 0x800, v246\n"
    "ds_read_b128 v[120:123], v250\n"
    "ds_read_b128 v[124:127], v250 offset:4096\n"
    "s_waitcnt vmcnt(6) lgkmcnt(2)\n"
    "v_mfma_f32_32x32x16_bf16 v[0:15], v[36:39], v[116:119], v[0:15]\n"
    "v_xor_b32 v250, 0xc00, v246\n"
    "ds_read_b128 v[224:227], v250\n"
    "ds_read_b128 v[228:231], v250 offset:4096\n"
    "ds_read_b128 v[232:235], v246\n"
    "ds_read_b128 v[236:239], v246 offset:4096\n"
    "s_mov_b64 exec, 0xffff\n"
    "buffer_load_dwordx2 v[242:243], v253, s[20:23], s66 offen sc1\n"
    "s_mov_b64 exec, -1\n"
    "v_mul_f32 v64, v16, v64\n"
    "v_mul_f32 v65, v17, v65\n"
    "v_cvt_pk_bf16_f32 v64, v64, v65\n"
    "v_mul_f32 v66, v18, v66\n"
    "v_mul_f32 v67, v19, v67\n"
    "v_cvt_pk_bf16_f32 v65, v66, v67\n"
    "v_mul_f32 v68, v20, v68\n"
    "v_mul_f32 v69, v21, v69\n"
    "v_cvt_pk_bf16_f32 v66, v68, v69\n"
    "v_mul_f32 v70, v22, v70\n"
    "v_mul_f32 v71, v23, v71\n"
    "v_cvt_pk_bf16_f32 v67, v70, v71\n"
    "buffer_store_dwordx4 v[64:67], v244, s[20:23], s57 offen sc1\n"
    "v_mul_f32 v72, v24, v72\n"
    "v_mul_f32 v73, v25, v73\n"
    "v_cvt_pk_bf16_f32 v68, v72, v73\n"
    "v_mul_f32 v74, v26, v74\n"
    "v_mul_f32 v75, v27, v75\n"
    "v_cvt_pk_bf16_f32 v69, v74, v75\n"
    "v_mul_f32 v76, v28, v76\n"
    "v_mul_f32 v77, v29, v77\n"
    "v_cvt_pk_bf16_f32 v70, v76, v77\n"
    "v_mul_f32 v78, v30, v78\n"
    "v_mul_f32 v79, v31, v79\n"
    "v_cvt_pk_bf16_f32 v71, v78, v79\n"
    "buffer_store_dwordx4 v[68:71], v244, s[20:23], s57 offen offset:1024 sc1\n"
    ".Lk_if22_end:\n"
    "s_cmp_lg_u32 s26, 0\n"
    "s_cbranch_scc0 .Lk_if27\n"
    "s_setprio 3\n"
    "s_waitcnt lgkmcnt(5)\n"
    "v_mfma_f32_32x32x16_bf16 v[16:31], v[32:35], v[120:123], 0\n"
    "v_exp_f32 v0, v0\n"
    "v_exp_f32 v1, v1\n"
    "v_exp_f32 v2, v2\n"
    "v_exp_f32 v3, v3\n"
    "v_cvt_pk_bf16_f32 v96, v0, v1\n"
    "v_exp_f32 v4, v4\n"
    "v_cvt_pk_bf16_f32 v97, v2, v3\n"
    "s_waitcnt lgkmcnt(4)\n"
    "v_mfma_f32_32x32x16_bf16 v[16:31], v[36:39], v[124:127], v[16:31]\n"
    "v_exp_f32 v5, v5\n"
    "v_exp_f32 v6, v6\n"
    "v_exp_f32 v7, v7\n"
    "v_cvt_pk_bf16_f32 v98, v4, v5\n"
    "s_setprio 2\n"
    "v_exp_f32 v8, v8\n"
    "v_cvt_pk_bf16_f32 v99, v6, v7\n"
    "v_exp_f32 v9, v9\n"
    "v_exp_f32 v10, v10\n"
    "s_waitcnt vmcnt(8)\n"
    "v_mfma_f32_32x32x16_bf16 v[160:175], v[48:51], v[96:99], 0\n"
    "v_exp_f32 v11, v11\n"
    "v_cvt_pk_bf16_f32 v100, v8, v9\n"
    "v_exp_f32 v12, v12\n"
    "v_cvt_pk_bf16_f32 v101, v10, v11\n"
    "v_exp_f32 v13, v13\n"
    "v_exp_f32 v14, v14\n"
    "v_exp_f32 v15, v15\n"
    "v_cvt_pk_bf16_f32 v102, v12, v13\n"
    "v_cvt_pk_bf16_f32 v103, v14, v15\n"
    "s_setprio 1\n"
    "s_waitcnt lgkmcnt(3)\n"
    "v_mfma_f32_32x32x16_bf16 v[0:15], v[32:35], v[224:227], 0\n"
    "v_exp_f32 v16, v16\n"
    "v_exp_f32 v17, v17\n"
    "v_exp_f32 v18, v18\n"
    "s_waitcnt vmcnt(7)\n"
    "v_mfma_f32_32x32x16_bf16 v[160:175], v[52:55], v[100:103], v[160:175]\n"
    "v_exp_f32 v19, v19\n"
    "v_cvt_pk_bf16_f32 v96, v16, v17\n"
    "v_exp_f32 v20, v20\n"
    "v_cvt_pk_bf16_f32 v97, v18, v19\n"
    "s_waitcnt lgkmcnt(2)\n"
    "v_mfma_f32_32x32x16_bf16 v[0:15], v[36:39], v[228:231], v[0:15]\n"
    "v_exp_f32 v21, v21\n"
    "v_exp_f32 v22, v22\n"
    "v_exp_f32 v23, v23\n"
    "v_cvt_pk_bf16_f32 v98, v20, v21\n"
    "s_setprio 0\n"
    "v_exp_f32 v24, v24\n"
    "v_cvt_pk_bf16_f32 v99, v22, v23\n"
    "v_exp_f32 v25, v25\n"
    "v_exp_f32 v26, v26\n"
    "v_mfma_f32_32x32x16_bf16 v[176:191], v[48:51], v[96:99], 0\n"
    "v_exp_f32 v27, v27\n"
    "v_cvt_pk_bf16_f32 v100, v24, v25\n"
    "v_exp_f32 v28, v28\n"
    "v_cvt_pk_bf16_f32 v101, v26, v27\n"
    "v_exp_f32 v29, v29\n"
    "v_exp_f32 v30, v30\n"
    "v_exp_f32 v31, v31\n"
    "v_cvt_pk_bf16_f32 v102, v28, v29\n"
    "v_cvt_pk_bf16_f32 v103, v30, v31\n"
    "s_waitcnt vmcnt(2)\n"
    "v_cmp_ne_u64 vcc, v[242:243], v[240:241]\n"
    "s_nop 1\n"
    "s_and_b32 s45, vcc_lo, 0xffff\n"
    "s_cbranch_scc0 .Lk_if24\n"
    "s_mov_b32 s70, 0x40000\n"
    ".Lk_lp23:\n"
    "s_sleep 1\n"
    "s_mov_b64 exec, 0xffff\n"
    "buffer_load_dwordx2 v[242:243], v253, s[20:23], s66 offen sc1\n"
    "s_mov_b64 exec, -1\n"
    "s_waitcnt vmcnt(0)\n"
    "v_cmp_ne_u64 vcc, v[242:243], v[240:241]\n"
    "s_sub_u32 s70, s70, 1\n"
    "s_nop 0\n"
    "s_cselect_b64 s[38:39], 0, vcc\n"
    "s_and_b32 s45, s38, 0xffff\n"
    "s_cbranch_scc1 .Lk_lp23\n"
    ".Lk_if24:\n"
    "s_setprio 3\n"
    "s_waitcnt lgkmcnt(1)\n"
    "v_mfma_f32_32x32x16_bf16 v[16:31], v[32:35], v[232:235], 0\n"
    "v_exp_f32 v0, v0\n"
    "v_exp_f32 v1, v1\n"
    "v_exp_f32 v2, v2\n"
    "v_mfma_f32_32x32x16_bf16 v[176:191], v[52:55], v[100:103], v[176:191]\n"
    "s_xor_b32 s47, s61, 0x400\n"
    "buffer_load_dwordx4 v[32:35], v245, s[20:23], s47 offen sc1\n"
    "v_exp_f32 v3, v3\n"
    "v_cvt_pk_bf16_f32 v96, v0, v1\n"
    "v_exp_f32 v4, v4\n"
    "v_cvt_pk_bf16_f32 v97, v2, v3\n"
    "s_waitcnt lgkmcnt(0)\n"
    "v_mfma_f32_32x32x16_bf16 v[16:31], v[36:39], v[236:239], v[16:31]\n"
    "s_xor_b32 s47, s62, 0x400\n"
    "buffer_load_dwordx4 v[36:39], v245, s[20:23], s47 offen sc1\n"
    "v_exp_f32 v5, v5\n"
    "v_exp_f32 v6, v6\n"
    "v_exp_f32 v7, v7\n"
    "v_cvt_pk_bf16_f32 v98, v4, v5\n"
    "s_setprio 2\n"
    "v_exp_f32 v8, v8\n"
    "v_cvt_pk_bf16_f32 v99, v6, v7\n"
    "v_exp_f32 v9, v9\n"
    "v_exp_f32 v10, v10\n"
    "v_mfma_f32_32x32x16_bf16 v[192:207], v[48:51], v[96:99], 0\n"
    "buffer_load_dwordx4 v[128:131], v244, s[16:19], s64 offen sc1\n"
    "v_exp_f32 v11, v11\n"
    "v_cvt_pk_bf16_f32 v100, v8, v9\n"
    "v_exp_f32 v12, v12\n"
    "v_cvt_pk_bf16_f32 v101, v10, v11\n"
    "s_add_u32 s64, s64, 0x4000\n"
    "buffer_load_dwordx4 v[132:135], v244, s[16:19], s64 offen sc1\n"
    "v_exp_f32 v13, v13\n"
    "v_exp_f32 v14, v14\n"
    "v_exp_f32 v15, v15\n"
    "v_cvt_pk_bf16_f32 v102, v12, v13\n"
    "v_cvt_pk_bf16_f32 v103, v14, v15\n"
    "s_setprio 1\n"
    "v_mfma_f32_32x32x16_bf16 v[0:15], v[40:43], v[112:115], 0\n"
    "s_add_u32 s59, s59, 0x40000\n"
    "s_waitcnt vmcnt(4)\n"
    "s_mov_b64 exec, 1\n"
    "buffer_store_dwordx2 v[240:241], off, s[20:23], s59 sc1\n"
    "s_mov_b64 exec, -1\n"
    "v_exp_f32 v16, v16\n"
    "v_exp_f32 v17, v17\n"
    "v_exp_f32 v18, v18\n"
    "v_mfma_f32_32x32x16_bf16 v[192:207], v[52:55], v[100:103], v[192:207]\n"
    "s_add_u32 s64, s64, 0x4000\n"
    "buffer_load_dwordx4 v[136:139], v244, s[16:19], s64 offen sc1\n"
    "v_exp_f32 v19, v19\n"
    "v_cvt_pk_bf16_f32 v96, v16, v17\n"
    "v_exp_f32 v20, v20\n"
    "v_cvt_pk_bf16_f32 v97, v18, v19\n"
    "v_mfma_f32_32x32x16_bf16 v[0:15], v[44:47], v[116:119], v[0:15]\n"
    "v_exp_f32 v21, v21\n"
    "v_exp_f32 v22, v22\n"
    "v_exp_f32 v23, v23\n"
    "v_cvt_pk_bf16_f32 v98, v20, v21\n"
    "s_setprio 0\n"
    "v_exp_f32 v24, v24\n"
    "v_cvt_pk_bf16_f32 v99, v22, v23\n"
    "v_exp_f32 v25, v25\n"
    "v_exp_f32 v26, v26\n"
    "v_mfma_f32_32x32x16_bf16 v[208:223], v[48:51], v[96:99], 0\n"
    "v_exp_f32 v27, v27\n"
    "v_cvt_pk_bf16_f32 v100, v24, v25\n"
    "v_exp_f32 v28, v28\n"
    "v_cvt_pk_bf16_f32 v101, v26, v27\n"
    "v_exp_f32 v29, v29\n"
    "v_exp_f32 v30, v30\n"
    "v_exp_f32 v31, v31\n"
    "v_cvt_pk_bf16_f32 v102, v28, v29\n"
    "v_cvt_pk_bf16_f32 v103, v30, v31\n"
    "s_setprio 3\n"
    "v_mfma_f32_32x32x16_bf16 v[16:31], v[40:43], v[120:123], 0\n"
    "v_exp_f32 v0, v0\n"
    "v_exp_f32 v1, v1\n"
    "v_exp_f32 v2, v2\n"
    "v_mfma_f32_32x32x16_bf16 v[208:223], v[52:55], v[100:103], v[208:223]\n"
    "s_xor_b32 s47, s63, 0x2000\n"
    "s_mov_b64 exec, s[36:37]\n"
    "buffer_load_dwordx4 v[48:51], v254, s[20:23], s47 offen sc1\n"
    "buffer_load_dwordx4 v[52:55], v254, s[20:23], s47 offen offset:2048 sc1\n"
    "s_mov_b64 exec, -1\n"
    "v_exp_f32 v3, v3\n"
    "v_cvt_pk_bf16_f32 v96, v0, v1\n"
    "v_exp_f32 v4, v4\n"
    "v_cvt_pk_bf16_f32 v97, v2, v3\n"
    "v_mfma_f32_32x32x16_bf16 v[16:31], v[44:47], v[124:127], v[16:31]\n"
    "s_add_u32 s64, s64, 0xffff8000\n"
    "s_xor_b32 s64, s64, 0x1000\n"
    "buffer_load_dwordx4 v[140:143], v244, s[16:19], s64 offen sc1\n"
    "v_exp_f32 v5, v5\n"
    "v_exp_f32 v6, v6\n"
    "v_exp_f32 v7, v7\n"
    "v_cvt_pk_bf16_f32 v98, v4, v5\n"
    "s_setprio 2\n"
    "ds_read_b64_tr_b16 v[104:105], v251 offset:57344\n"
    "ds_read_b64_tr_b16 v[106:107], v251 offset:58368\n"
    "v_exp_f32 v8, v8\n"
    "v_cvt_pk_bf16_f32 v99, v6, v7\n"
    "v_exp_f32 v9, v9\n"
    "v_exp_f32 v10, v10\n"
    "v_mfma_f32_32x32x16_bf16 v[160:175], v[56:59], v[96:99], v[160:175]\n"
    "s_add_u32 s64, s64, 0x4000\n"
    "buffer_load_dwordx4 v[144:147], v244, s[16:19], s64 offen sc1\n"
    "v_exp_f32 v11, v11\n"
    "v_cvt_pk_bf16_f32 v100, v8, v9\n"
    "v_exp_f32 v12, v12\n"
    "v_cvt_pk_bf16_f32 v101, v10, v11\n"
    "ds_read_b64_tr_b16 v[108:109], v252 offset:57344\n"
    "ds_read_b64_tr_b16 v[110:111], v252 offset:58368\n"
    "v_exp_f32 v13, v13\n"
    "v_exp_f32 v14, v14\n"
    "v_exp_f32 v15, v15\n"
    "v_cvt_pk_bf16_f32 v102, v12, v13\n"
    "v_cvt_pk_bf16_f32 v103, v14, v15\n"
    "s_setprio 1\n"
    "v_mfma_f32_32x32x16_bf16 v[0:15], v[40:43], v[224:227], 0\n"
    "v_exp_f32 v16, v16\n"
    "v_exp_f32 v17, v17\n"
    "v_exp_f32 v18, v18\n"
    "v_mfma_f32_32x32x16_bf16 v[160:175], v[60:63], v[100:103], v[160:175]\n"
    "s_add_u32 s64, s64, 0x4000\n"
    "buffer_load_dwordx4 v[148:151], v244, s[16:19], s64 offen sc1\n"
    "v_exp_f32 v19, v19\n"
    "v_cvt_pk_bf16_f32 v96, v16, v17\n"
    "v_exp_f32 v20, v20\n"
    "v_cvt_pk_bf16_f32 v97, v18, v19\n"
    "v_mfma_f32_32x32x16_bf16 v[0:15], v[44:47], v[228:231], v[0:15]\n"
    "s_xor_b32 s60, s73, 3\n"
    "s_mul_i32 s60, s60, 0xe000\n"
    "s_add_u32 m0, s50, 0x8000\n"
    "s_add_u32 s46, s60, 0\n"
    "buffer_load_dwordx4 v249, s[24:27], s46 offen lds\n"
    "v_exp_f32 v21, v21\n"
    "v_exp_f32 v22, v22\n"
    "v_exp_f32 v23, v23\n"
    "v_cvt_pk_bf16_f32 v98, v20, v21\n"
    "s_setprio 0\n"
    "s_waitcnt lgkmcnt(0)\n"
    "s_waitcnt vmcnt(9)\n"
    "v_mfma_f32_32x32x16_bf16 v[64:79], v[104:107], v[128:131], 0\n"
    "v_add_u32 v251, 0x8000, v251\n"
    "ds_read_b64_tr_b16 v[104:105], v251 offset:49152\n"
    "ds_read_b64_tr_b16 v[106:107], v251 offset:50176\n"
    "v_exp_f32 v24, v24\n"
    "v_cvt_pk_bf16_f32 v99, v22, v23\n"
    "v_exp_f32 v25, v25\n"
    "v_exp_f32 v26, v26\n"
    "v_mfma_f32_32x32x16_bf16 v[176:191], v[56:59], v[96:99], v[176:191]\n"
    "s_add_u32 m0, s50, 0xc000\n"
    "s_add_u32 s46, s60, 0x38000\n"
    "buffer_load_dwordx4 v249, s[24:27], s46 offen lds\n"
    "v_exp_f32 v27, v27\n"
    "v_cvt_pk_bf16_f32 v100, v24, v25\n"
    "v_exp_f32 v28, v28\n"
    "v_cvt_pk_bf16_f32 v101, v26, v27\n"
    "v_mfma_f32_32x32x16_bf16 v[80:95], v[108:111], v[128:131], 0\n"
    "v_add_u32 v252, 0x8000, v252\n"
    "ds_read_b64_tr_b16 v[108:109], v252 offset:49152\n"
    "ds_read_b64_tr_b16 v[110:111], v252 offset:50176\n"
    "s_add_u32 m0, s50, 0x20000\n"
    "s_add_u32 s46, s60, 0x70000\n"
    "buffer_load_dwordx4 v249, s[24:27], s46 offen lds\n"
    "v_exp_f32 v29, v29\n"
    "v_exp_f32 v30, v30\n"
    "v_exp_f32 v31, v31\n"
    "v_cvt_pk_bf16_f32 v102, v28, v29\n"
    "v_cvt_pk_bf16_f32 v103, v30, v31\n"
    "s_setprio 3\n"
    "v_mfma_f32_32x32x16_bf16 v[16:31], v[40:43], v[232:235], 0\n"
    "v_exp_f32 v0, v0\n"
    "v_exp_f32 v1, v1\n"
    "v_exp_f32 v2, v2\n"
    "v_mfma_f32_32x32x16_bf16 v[176:191], v[60:63], v[100:103], v[176:191]\n"
    "s_xor_b32 s47, s61, 0x600\n"
    "buffer_load_dwordx4 v[40:43], v245, s[20:23], s47 offen sc1\n"
    "v_exp_f32 v3, v3\n"
    "v_cvt_pk_bf16_f32 v96, v0, v1\n"
    "v_exp_f32 v4, v4\n"
    "v_cvt_pk_bf16_f32 v97, v2, v3\n"
    "v_mfma_f32_32x32x16_bf16 v[16:31], v[44:47], v[236:239], v[16:31]\n"
    "s_xor_b32 s47, s62, 0x600\n"
    "buffer_load_dwordx4 v[44:47], v245, s[20:23], s47 offen sc1\n"
    "v_exp_f32 v5, v5\n"
    "v_exp_f32 v6, v6\n"
    "v_exp_f32 v7, v7\n"
    "v_cvt_pk_bf16_f32 v98, v4, v5\n"
    "s_setprio 2\n"
    "s_waitcnt lgkmcnt(0)\n"
    "s_waitcnt vmcnt(12)\n"
    "v_mfma_f32_32x32x16_bf16 v[64:79], v[104:107], v[132:135], v[64:79]\n"
    "ds_read_b64_tr_b16 v[104:105], v251 offset:57344\n"
    "ds_read_b64_tr_b16 v[106:107], v251 offset:58368\n"
    "v_exp_f32 v8, v8\n"
    "v_cvt_pk_bf16_f32 v99, v6, v7\n"
    "v_exp_f32 v9, v9\n"
    "v_exp_f32 v10, v10\n"
    "v_mfma_f32_32x32x16_bf16 v[192:207], v[56:59], v[96:99], v[192:207]\n"
    "s_add_u32 s64, s64, 0xffff8000\n"
    "s_xor_b32 s64, s64, 0x3000\n"
    "buffer_load_dwordx4 v[152:155], v244, s[16:19], s64 offen sc1\n"
    "v_exp_f32 v11, v11\n"
    "v_cvt_pk_bf16_f32 v100, v8, v9\n"
    "v_exp_f32 v12, v12\n"
    "v_cvt_pk_bf16_f32 v101, v10, v11\n"
    "v_mfma_f32_32x32x16_bf16 v[80:95], v[108:111], v[132:135], v[80:95]\n"
    "ds_read_b64_tr_b16 v[108:109], v252 offset:57344\n"
    "ds_read_b64_tr_b16 v[110:111], v252 offset:58368\n"
    "s_xor_b32 s60, s73, 0\n"
    "s_mul_i32 s60, s60, 0xe000\n"
    "s_add_u32 m0, s50, 0x18000\n"
    "s_add_u32 s46, s60, 0xa8000\n"
    "buffer_load_dwordx4 v249, s[24:27], s46 offen lds\n"
    "v_exp_f32 v13, v13\n"
    "v_exp_f32 v14, v14\n"
    "v_exp_f32 v15, v15\n"
    "v_cvt_pk_bf16_f32 v102, v12, v13\n"
    "v_cvt_pk_bf16_f32 v103, v14, v15\n"
    "s_setprio 1\n"
    "v_mfma_f32_32x32x16_bf16 v[0:15], v[32:35], v[112:115], 0\n"
    "v_exp_f32 v16, v16\n"
    "v_exp_f32 v17, v17\n"
    "v_exp_f32 v18, v18\n"
    "v_mfma_f32_32x32x16_bf16 v[192:207], v[60:63], v[100:103], v[192:207]\n"
    "s_add_u32 s64, s64, 0x4000\n"
    "buffer_load_dwordx4 v[156:159], v244, s[16:19], s64 offen sc1\n"
    "v_exp_f32 v19, v19\n"
    "v_cvt_pk_bf16_f32 v96, v16, v17\n"
    "v_exp_f32 v20, v20\n"
    "v_cvt_pk_bf16_f32 v97, v18, v19\n"
    "v_mfma_f32_32x32x16_bf16 v[0:15], v[36:39], v[116:119], v[0:15]\n"
    "s_add_u32 m0, s50, 0x1a000\n"
    "s_add_u32 s46, s60, 0xe0000\n"
    "buffer_load_dwordx4 v249, s[24:27], s46 offen lds\n"
    "v_exp_f32 v21, v21\n"
    "v_exp_f32 v22, v22\n"
    "v_exp_f32 v23, v23\n"
    "v_cvt_pk_bf16_f32 v98, v20, v21\n"
    "s_setprio 0\n"
    "s_waitcnt lgkmcnt(0)\n"
    "s_waitcnt vmcnt(14)\n"
    "v_mfma_f32_32x32x16_bf16 v[64:79], v[104:107], v[136:139], v[64:79]\n"
    "v_add_u32 v251, 0xffff8000, v251\n"
    "ds_read_b64_tr_b16 v[104:105], v251\n"
    "ds_read_b64_tr_b16 v[106:107], v251 offset:1024\n"
    "v_exp_f32 v24, v24\n"
    "v_cvt_pk_bf16_f32 v99, v22, v23\n"
    "v_exp_f32 v25, v25\n"
    "v_exp_f32 v26, v26\n"
    "v_mfma_f32_32x32x16_bf16 v[208:223], v[56:59], v[96:99], v[208:223]\n"
    "s_add_u32 m0, s50, 0x1c000\n"
    "s_add_u32 s46, s60, 0x118000\n"
    "buffer_load_dwordx4 v249, s[24:27], s46 offen lds\n"
    "v_exp_f32 v27, v27\n"
    "v_cvt_pk_bf16_f32 v100, v24, v25\n"
    "v_exp_f32 v28, v28\n"
    "v_cvt_pk_bf16_f32 v101, v26, v27\n"
    "v_mfma_f32_32x32x16_bf16 v[80:95], v[108:111], v[136:139], v[80:95]\n"
    "v_add_u32 v252, 0xffff8000, v252\n"
    "ds_read_b64_tr_b16 v[108:109], v252\n"
    "ds_read_b64_tr_b16 v[110:111], v252 offset:1024\n"
    "s_add_u32 m0, s50, 0x1e000\n"
    "s_add_u32 s46, s60, 0x150000\n"
    "buffer_load_dwordx4 v249, s[24:27], s46 offen lds\n"
    "v_exp_f32 v29, v29\n"
    "v_exp_f32 v30, v30\n"
    "v_exp_f32 v31, v31\n"
    "v_cvt_pk_bf16_f32 v102, v28, v29\n"
    "v_cvt_pk_bf16_f32 v103, v30, v31\n"
    "s_setprio 3\n"
    "v_mfma_f32_32x32x16_bf16 v[16:31], v[32:35], v[120:123], 0\n"
    "v_exp_f32 v0, v0\n"
    "v_exp_f32 v1, v1\n"
    "v_exp_f32 v2, v2\n"
    "v_mfma_f32_32x32x16_bf16 v[208:223], v[60:63], v[100:103], v[208:223]\n"
    "s_xor_b32 s47, s63, 0x3000\n"
    "s_mov_b64 exec, s[36:37]\n"
    "buffer_load_dwordx4 v[56:59], v254, s[20:23], s47 offen sc1\n"
    "buffer_load_dwordx4 v[60:63], v254, s[20:23], s47 offen offset:2048 sc1\n"
    "s_mov_b64 exec, -1\n"
    "v_exp_f32 v3, v3\n"
    "v_cvt_pk_bf16_f32 v96, v0, v1\n"
    "v_exp_f32 v4, v4\n"
    "v_cvt_pk_bf16_f32 v97, v2, v3\n"
    "v_mfma_f32_32x32x16_bf16 v[16:31], v[36:39], v[124:127], v[16:31]\n"
    "s_add_u32 s64, s64, 0x4000\n"
    "buffer_load_dwordx4 v[128:131], v244, s[16:19], s64 offen sc1\n"
    "v_exp_f32 v5, v5\n"
    "v_exp_f32 v6, v6\n"
    "v_exp_f32 v7, v7\n"
    "v_cvt_pk_bf16_f32 v98, v4, v5\n"
    "s_setprio 2\n"
    "s_waitcnt lgkmcnt(0)\n"
    "s_waitcnt vmcnt(16)\n"
    "v_mfma_f32_32x32x16_bf16 v[64:79], v[104:107], v[140:143], v[64:79]\n"
    "ds_read_b64_tr_b16 v[104:105], v251 offset:8192\n"
    "ds_read_b64_tr_b16 v[106:107], v251 offset:9216\n"
    "v_exp_f32 v8, v8\n"
    "v_cvt_pk_bf16_f32 v99, v6, v7\n"
    "v_exp_f32 v9, v9\n"
    "v_exp_f32 v10, v10\n"
    "v_mfma_f32_32x32x16_bf16 v[160:175], v[48:51], v[96:99], v[160:175]\n"
    "v_exp_f32 v11, v11\n"
    "v_cvt_pk_bf16_f32 v100, v8, v9\n"
    "v_exp_f32 v12, v12\n"
    "v_cvt_pk_bf16_f32 v101, v10, v11\n"
    "v_mfma_f32_32x32x16_bf16 v[80:95], v[108:111], v[140:143], v[80:95]\n"
    "ds_read_b64_tr_b16 v[108:109], v252 offset:8192\n"
    "ds_read_b64_tr_b16 v[110:111], v252 offset:9216\n"
    "v_exp_f32 v13, v13\n"
    "v_exp_f32 v14, v14\n"
    "v_exp_f32 v15, v15\n"
    "v_cvt_pk_bf16_f32 v102, v12, v13\n"
    "v_cvt_pk_bf16_f32 v103, v14, v15\n"
    "s_waitcnt vmcnt(11) lgkmcnt(0)\n"
    "s_barrier\n"
    "s_setprio 1\n"
    "v_mfma_f32_32x32x16_bf16 v[0:15], v[32:35], v[224:227], 0\n"
    "s_add_u32 s66, s66, 0x40000\n"
    "s_mov_b64 exec, 0xffff\n"
    "buffer_load_dwordx2 v[242:243], v253, s[20:23], s66 offen sc1\n"
    "s_mov_b64 exec, -1\n"
    "v_exp_f32 v16, v16\n"
    "v_exp_f32 v17, v17\n"
    "v_exp_f32 v18, v18\n"
    "v_mfma_f32_32x32x16_bf16 v[160:175], v[52:55], v[100:103], v[160:175]\n"
    "s_add_u32 s64, s64, 0xffff8000\n"
    "s_xor_b32 s64, s64, 0x1000\n"
    "buffer_load_dwordx4 v[132:135], v244, s[16:19], s64 offen sc1\n"
    "v_exp_f32 v19, v19\n"
    "v_cvt_pk_bf16_f32 v96, v16, v17\n"
    "v_exp_f32 v20, v20\n"
    "v_cvt_pk_bf16_f32 v97, v18, v19\n"
    "v_mfma_f32_32x32x16_bf16 v[0:15], v[36:39], v[228:231], v[0:15]\n"
    "s_xor_b32 s60, s73, 1\n"
    "s_mul_i32 s60, s60, 0xe000\n"
    "s_add_u32 m0, s50, 0x14000\n"
    "s_add_u32 s46, s60, 0xa8000\n"
    "buffer_load_dwordx4 v249, s[24:27], s46 offen lds\n"
    "v_exp_f32 v21, v21\n"
    "v_exp_f32 v22, v22\n"
    "v_exp_f32 v23, v23\n"
    "v_cvt_pk_bf16_f32 v98, v20, v21\n"
    "s_setprio 0\n"
    "s_waitcnt lgkmcnt(0)\n"
    "v_mfma_f32_32x32x16_bf16 v[64:79], v[104:107], v[144:147], v[64:79]\n"
    "ds_read_b64_tr_b16 v[104:105], v251 offset:16384\n"
    "ds_read_b64_tr_b16 v[106:107], v251 offset:17408\n"
    "v_exp_f32 v24, v24\n"
    "v_cvt_pk_bf16_f32 v99, v22, v23\n"
    "v_exp_f32 v25, v25\n"
    "v_exp_f32 v26, v26\n"
    "v_mfma_f32_32x32x16_bf16 v[176:191], v[48:51], v[96:99], v[176:191]\n"
    "s_add_u32 m0, s50, 0x16000\n"
    "s_add_u32 s46, s60, 0xe0000\n"
    "buffer_load_dwordx4 v249, s[24:27], s46 offen lds\n"
    "v_exp_f32 v27, v27\n"
    "v_cvt_pk_bf16_f32 v100, v24, v25\n"
    "v_exp_f32 v28, v28\n"
    "v_cvt_pk_bf16_f32 v101, v26, v27\n"
    "v_mfma_f32_32x32x16_bf16 v[80:95], v[108:111], v[144:147], v[80:95]\n"
    "ds_read_b64_tr_b16 v[108:109], v252 offset:16384\n"
    "ds_read_b64_tr_b16 v[110:111], v252 offset:17408\n"
    "v_exp_f32 v29, v29\n"
    "v_exp_f32 v30, v30\n"
    "v_exp_f32 v31, v31\n"
    "v_cvt_pk_bf16_f32 v102, v28, v29\n"
    "v_cvt_pk_bf16_f32 v103, v30, v31\n"
    "s_setprio 3\n"
    "v_mfma_f32_32x32x16_bf16 v[16:31], v[32:35], v[232:235], 0\n"
    "v_exp_f32 v0, v0\n"
    "v_exp_f32 v1, v1\n"
    "v_exp_f32 v2, v2\n"
    "v_mfma_f32_32x32x16_bf16 v[176:191], v[52:55], v[100:103], v[176:191]\n"
    "s_xor_b32 s47, s61, 0x800\n"
    "buffer_load_dwordx4 v[32:35], v245, s[20:23], s47 offen sc1\n"
    "v_exp_f32 v3, v3\n"
    "v_cvt_pk_bf16_f32 v96, v0, v1\n"
    "v_exp_f32 v4, v4\n"
    "v_cvt_pk_bf16_f32 v97, v2, v3\n"
    "v_mfma_f32_32x32x16_bf16 v[16:31], v[36:39], v[236:239], v[16:31]\n"
    "s_xor_b32 s47, s62, 0x800\n"
    "buffer_load_dwordx4 v[36:39], v245, s[20:23], s47 offen sc1\n"
    "v_exp_f32 v5, v5\n"
    "v_exp_f32 v6, v6\n"
    "v_exp_f32 v7, v7\n"
    "v_cvt_pk_bf16_f32 v98, v4, v5\n"
    "s_setprio 2\n"
    "s_waitcnt lgkmcnt(0)\n"
    "v_mfma_f32_32x32x16_bf16 v[64:79], v[104:107], v[148:151], v[64:79]\n"
    "ds_read_b64_tr_b16 v[104:105], v251 offset:24576\n"
    "ds_read_b64_tr_b16 v[106:107], v251 offset:25600\n"
    "v_exp_f32 v8, v8\n"
    "v_cvt_pk_bf16_f32 v99, v6, v7\n"
    "v_exp_f32 v9, v9\n"
    "v_exp_f32 v10, v10\n"
    "v_mfma_f32_32x32x16_bf16 v[192:207], v[48:51], v[96:99], v[192:207]\n"
    "s_add_u32 s64, s64, 0x4000\n"
    "buffer_load_dwordx4 v[136:139], v244, s[16:19], s64 offen sc1\n"
    "v_exp_f32 v11, v11\n"
    "v_cvt_pk_bf16_f32 v100, v8, v9\n"
    "v_exp_f32 v12, v12\n"
    "v_cvt_pk_bf16_f32 v101, v10, v11\n"
    "v_mfma_f32_32x32x16_bf16 v[80:95], v[108:111], v[148:151], v[80:95]\n"
    "ds_read_b64_tr_b16 v[108:109], v252 offset:24576\n"
    "ds_read_b64_tr_b16 v[110:111], v252 offset:25600\n"
    "v_exp_f32 v13, v13\n"
    "v_exp_f32 v14, v14\n"
    "v_exp_f32 v15, v15\n"
    "v_cvt_pk_bf16_f32 v102, v12, v13\n"
    "v_cvt_pk_bf16_f32 v103, v14, v15\n"
    "s_setprio 1\n"
    "s_waitcnt vmcnt(17)\n"
    "v_mfma_f32_32x32x16_bf16 v[0:15], v[40:43], v[112:115], 0\n"
    "v_exp_f32 v16, v16\n"
    "v_exp_f32 v17, v17\n"
    "v_exp_f32 v18, v18\n"
    "v_mfma_f32_32x32x16_bf16 v[192:207], v[52:55], v[100:103], v[192:207]\n"
    "s_add_u32 s64, s64, 0x4000\n"
    "buffer_load_dwordx4 v[140:143], v244, s[16:19], s64 offen sc1\n"
    "v_exp_f32 v19, v19\n"
    "v_cvt_pk_bf16_f32 v96, v16, v17\n"
    "v_exp_f32 v20, v20\n"
    "v_cvt_pk_bf16_f32 v97, v18, v19\n"
    "s_waitcnt vmcnt(17)\n"
    "v_mfma_f32_32x32x16_bf16 v[0:15], v[44:47], v[116:119], v[0:15]\n"
    "s_add_u32 m0, s50, 0x2000\n"
    "s_add_u32 s46, s60, 0x118000\n"
    "buffer_load_dwordx4 v249, s[24:27], s46 offen lds\n"
    "v_exp_f32 v21, v21\n"
    "v_exp_f32 v22, v22\n"
    "v_exp_f32 v23, v23\n"
    "v_cvt_pk_bf16_f32 v98, v20, v21\n"
    "s_setprio 0\n"
    "s_waitcnt lgkmcnt(0)\n"
    "s_waitcnt vmcnt(17)\n"
    "v_mfma_f32_32x32x16_bf16 v[64:79], v[104:107], v[152:155], v[64:79]\n"
    "v_add_u32 v251, 0x4000, v251\n"
    "ds_read_b64_tr_b16 v[104:105], v251 offset:49152\n"
    "ds_read_b64_tr_b16 v[106:107], v251 offset:50176\n"
    "v_exp_f32 v24, v24\n"
    "v_cvt_pk_bf16_f32 v99, v22, v23\n"
    "v_exp_f32 v25, v25\n"
    "v_exp_f32 v26, v26\n"
    "v_mfma_f32_32x32x16_bf16 v[208:223], v[48:51], v[96:99], v[208:223]\n"
    "s_add_u32 m0, s50, 0xe000\n"
    "s_add_u32 s46, s60, 0x150000\n"
    "buffer_load_dwordx4 v249, s[24:27], s46 offen lds\n"
    "v_exp_f32 v27, v27\n"
    "v_cvt_pk_bf16_f32 v100, v24, v25\n"
    "v_exp_f32 v28, v28\n"
    "v_cvt_pk_bf16_f32 v101, v26, v27\n"
    "v_mfma_f32_32x32x16_bf16 v[80:95], v[108:111], v[152:155], v[80:95]\n"
    "v_add_u32 v252, 0x4000, v252\n"
    "ds_read_b64_tr_b16 v[108:109], v252 offset:49152\n"
    "ds_read_b64_tr_b16 v[110:111], v252 offset:50176\n"
    "v_exp_f32 v29, v29\n"
    "v_exp_f32 v30, v30\n"
    "v_exp_f32 v31, v31\n"
    "v_cvt_pk_bf16_f32 v102, v28, v29\n"
    "v_cvt_pk_bf16_f32 v103, v30, v31\n"
    "s_waitcnt vmcnt(9)\n"
    "v_cmp_ne_u64 vcc, v[242:243], v[240:241]\n"
    "s_nop 1\n"
    "s_and_b32 s45, vcc_lo, 0xffff\n"
    "s_cbranch_scc0 .Lk_if26\n"
    "s_mov_b32 s70, 0x40000\n"
    ".Lk_lp25:\n"
    "s_sleep 1\n"
    "s_mov_b64 exec, 0xffff\n"
    "buffer_load_dwordx2 v[242:243], v253, s[20:23], s66 offen sc1\n"
    "s_mov_b64 exec, -1\n"
    "s_waitcnt vmcnt(0)\n"
    "v_cmp_ne_u64 vcc, v[242:243], v[240:241]\n"
    "s_sub_u32 s70, s70, 1\n"
    "s_nop 0\n"
    "s_cselect_b64 s[38:39], 0, vcc\n"
    "s_and_b32 s45, s38, 0xffff\n"
    "s_cbranch_scc1 .Lk_lp25\n"
    ".Lk_if26:\n"
    "s_setprio 3\n"
    "v_mfma_f32_32x32x16_bf16 v[16:31], v[40:43], v[120:123], 0\n"
    "v_exp_f32 v0, v0\n"
    "v_exp_f32 v1, v1\n"
    "v_exp_f32 v2, v2\n"
    "v_mfma_f32_32x32x16_bf16 v[208:223], v[52:55], v[100:103], v[208:223]\n"
    "s_xor_b32 s47, s63, 0x4000\n"
    "s_mov_b64 exec, s[36:37]\n"
    "buffer_load_dwordx4 v[48:51], v254, s[20:23], s47 offen sc1\n"
    "buffer_load_dwordx4 v[52:55], v254, s[20:23], s47 offen offset:2048 sc1\n"
    "s_mov_b64 exec, -1\n"
    "v_exp_f32 v3, v3\n"
    "v_cvt_pk_bf16_f32 v96, v0, v1\n"
    "v_exp_f32 v4, v4\n"
    "v_cvt_pk_bf16_f32 v97, v2, v3\n"
    "v_mfma_f32_32x32x16_bf16 v[16:31], v[44:47], v[124:127], v[16:31]\n"
    "s_add_u32 s64, s64, 0x4000\n"
    "s_xor_b32 s64, s64, 0x3000\n"
    "buffer_load_dwordx4 v[144:147], v244, s[16:19], s64 offen sc1\n"
    "v_exp_f32 v5, v5\n"
    "v_exp_f32 v6, v6\n"
    "v_exp_f32 v7, v7\n"
    "v_cvt_pk_bf16_f32 v98, v4, v5\n"
    "s_setprio 2\n"
    "s_waitcnt lgkmcnt(0)\n"
    "v_mfma_f32_32x32x16_bf16 v[64:79], v[104:107], v[156:159], v[64:79]\n"
    "ds_read_b64_tr_b16 v[104:105], v251 offset:57344\n"
    "ds_read_b64_tr_b16 v[106:107], v251 offset:58368\n"
    "v_exp_f32 v8, v8\n"
    "v_cvt_pk_bf16_f32 v99, v6, v7\n"
    "v_exp_f32 v9, v9\n"
    "v_exp_f32 v10, v10\n"
    "v_mfma_f32_32x32x16_bf16 v[160:175], v[56:59], v[96:99], v[160:175]\n"
    "s_xor_b32 s60, s73, 2\n"
    "s_mul_i32 s60, s60, 0xe000\n"
    "s_add_u32 m0, s50, 0xa000\n"
    "s_add_u32 s46, s60, 0xa8000\n"
    "buffer_load_dwordx4 v249, s[24:27], s46 offen lds\n"
    "v_exp_f32 v11, v11\n"
    "v_cvt_pk_bf16_f32 v100, v8, v9\n"
    "v_exp_f32 v12, v12\n"
    "v_cvt_pk_bf16_f32 v101, v10, v11\n"
    "v_mfma_f32_32x32x16_bf16 v[80:95], v[108:111], v[156:159], v[80:95]\n"
    "ds_read_b64_tr_b16 v[108:109], v252 offset:57344\n"
    "ds_read_b64_tr_b16 v[110:111], v252 offset:58368\n"
    "v_exp_f32 v13, v13\n"
    "v_exp_f32 v14, v14\n"
    "v_exp_f32 v15, v15\n"
    "v_cvt_pk_bf16_f32 v102, v12, v13\n"
    "v_cvt_pk_bf16_f32 v103, v14, v15\n"
    "s_setprio 1\n"
    "v_mfma_f32_32x32x16_bf16 v[0:15], v[40:43], v[224:227], 0\n"
    "v_exp_f32 v16, v16\n"
    "v_exp_f32 v17, v17\n"
    "v_exp_f32 v18, v18\n"
    "v_mfma_f32_32x32x16_bf16 v[160:175], v[60:63], v[100:103], v[160:175]\n"
    "s_add_u32 s64, s64, 0x4000\n"
    "buffer_load_dwordx4 v[148:151], v244, s[16:19], s64 offen sc1\n"
    "v_exp_f32 v19, v19\n"
    "v_cvt_pk_bf16_f32 v96, v16, v17\n"
    "v_exp_f32 v20, v20\n"
    "v_cvt_pk_bf16_f32 v97, v18, v19\n"
    "v_mfma_f32_32x32x16_bf16 v[0:15], v[44:47], v[228:231], v[0:15]\n"
    "v_exp_f32 v21, v21\n"
    "v_exp_f32 v22, v22\n"
    "v_exp_f32 v23, v23\n"
    "v_cvt_pk_bf16_f32 v98, v20, v21\n"
    "s_setprio 0\n"
    "s_waitcnt lgkmcnt(0)\n"
    "v_mfma_f32_32x32x16_bf16 v[64:79], v[104:107], v[128:131], v[64:79]\n"
    "ds_read_b64_tr_b16 v[104:105], v251 offset:16384\n"
    "ds_read_b64_tr_b16 v[106:107], v251 offset:17408\n"
    "v_exp_f32 v24, v24\n"
    "v_cvt_pk_bf16_f32 v99, v22, v23\n"
    "v_exp_f32 v25, v25\n"
    "v_exp_f32 v26, v26\n"
    "v_mfma_f32_32x32x16_bf16 v[176:191], v[56:59], v[96:99], v[176:191]\n"
    "v_exp_f32 v27, v27\n"
    "v_cvt_pk_bf16_f32 v100, v24, v25\n"
    "v_exp_f32 v28, v28\n"
    "v_cvt_pk_bf16_f32 v101, v26, v27\n"
    "v_mfma_f32_32x32x16_bf16 v[80:95], v[108:111], v[128:131], v[80:95]\n"
    "ds_read_b64_tr_b16 v[108:109], v252 offset:16384\n"
    "ds_read_b64_tr_b16 v[110:111], v252 offset:17408\n"
    "v_exp_f32 v29, v29\n"
    "v_exp_f32 v30, v30\n"
    "v_exp_f32 v31, v31\n"
    "v_cvt_pk_bf16_f32 v102, v28, v29\n"
    "v_cvt_pk_bf16_f32 v103, v30, v31\n"
    "s_setprio 3\n"
    "v_mfma_f32_32x32x16_bf16 v[16:31], v[40:43], v[232:235], 0\n"
    "v_exp_f32 v0, v0\n"
    "v_exp_f32 v1, v1\n"
    "v_exp_f32 v2, v2\n"
    "v_mfma_f32_32x32x16_bf16 v[176:191], v[60:63], v[100:103], v[176:191]\n"
    "s_xor_b32 s47, s61, 0xa00\n"
    "buffer_load_dwordx4 v[40:43], v245, s[20:23], s47 offen sc1\n"
    "v_exp_f32 v3, v3\n"
    "v_cvt_pk_bf16_f32 v96, v0, v1\n"
    "v_exp_f32 v4, v4\n"
    "v_cvt_pk_bf16_f32 v97, v2, v3\n"
    "v_mfma_f32_32x32x16_bf16 v[16:31], v[44:47], v[236:239], v[16:31]\n"
    "s_xor_b32 s47, s62, 0xa00\n"
    "buffer_load_dwordx4 v[44:47], v245, s[20:23], s47 offen sc1\n"
    "v_exp_f32 v5, v5\n"
    "v_exp_f32 v6, v6\n"
    "v_exp_f32 v7, v7\n"
    "v_cvt_pk_bf16_f32 v98, v4, v5\n"
    "s_setprio 2\n"
    "s_waitcnt lgkmcnt(0)\n"
    "s_waitcnt vmcnt(15)\n"
    "v_mfma_f32_32x32x16_bf16 v[64:79], v[104:107], v[132:135], v[64:79]\n"
    "ds_read_b64_tr_b16 v[104:105], v251 offset:32768\n"
    "ds_read_b64_tr_b16 v[106:107], v251 offset:33792\n"
    "v_exp_f32 v8, v8\n"
    "v_cvt_pk_bf16_f32 v99, v6, v7\n"
    "v_exp_f32 v9, v9\n"
    "v_exp_f32 v10, v10\n"
    "v_mfma_f32_32x32x16_bf16 v[192:207], v[56:59], v[96:99], v[192:207]\n"
    "s_add_u32 s64, s64, 0x4000\n"
    "buffer_load_dwordx4 v[152:155], v244, s[16:19], s64 offen sc1\n"
    "v_exp_f32 v11, v11\n"
    "v_cvt_pk_bf16_f32 v100, v8, v9\n"
    "v_exp_f32 v12, v12\n"
    "v_cvt_pk_bf16_f32 v101, v10, v11\n"
    "v_mfma_f32_32x32x16_bf16 v[80:95], v[108:111], v[132:135], v[80:95]\n"
    "ds_read_b64_tr_b16 v[108:109], v252 offset:32768\n"
    "ds_read_b64_tr_b16 v[110:111], v252 offset:33792\n"
    "v_exp_f32 v13, v13\n"
    "v_exp_f32 v14, v14\n"
    "v_exp_f32 v15, v15\n"
    "v_cvt_pk_bf16_f32 v102, v12, v13\n"
    "v_cvt_pk_bf16_f32 v103, v14, v15\n"
    "s_setprio 1\n"
    "s_waitcnt vmcnt(13)\n"
    "v_mfma_f32_32x32x16_bf16 v[0:15], v[32:35], v[112:115], 0\n"
    "v_exp_f32 v16, v16\n"
    "v_exp_f32 v17, v17\n"
    "v_exp_f32 v18, v18\n"
    "v_mfma_f32_32x32x16_bf16 v[192:207], v[60:63], v[100:103], v[192:207]\n"
    "s_add_u32 s64, s64, 0x4000\n"
    "buffer_load_dwordx4 v[156:159], v244, s[16:19], s64 offen sc1\n"
    "v_exp_f32 v19, v19\n"
    "v_cvt_pk_bf16_f32 v96, v16, v17\n"
    "v_exp_f32 v20, v20\n"
    "v_cvt_pk_bf16_f32 v97, v18, v19\n"
    "s_waitcnt vmcnt(13)\n"
    "v_mfma_f32_32x32x16_bf16 v[0:15], v[36:39], v[116:119], v[0:15]\n"
    "v_exp_f32 v21, v21\n"
    "v_exp_f32 v22, v22\n"
    "v_exp_f32 v23, v23\n"
    "v_cvt_pk_bf16_f32 v98, v20, v21\n"
    "s_setprio 0\n"
    "s_waitcnt lgkmcnt(0)\n"
    "s_waitcnt vmcnt(12)\n"
    "v_mfma_f32_32x32x16_bf16 v[64:79], v[104:107], v[136:139], v[64:79]\n"
    "v_add_u32 v251, 0xe000, v251\n"
    "ds_read_b64_tr_b16 v[104:105], v251 offset:57344\n"
    "ds_read_b64_tr_b16 v[106:107], v251 offset:58368\n"
    "v_exp_f32 v24, v24\n"
    "v_cvt_pk_bf16_f32 v99, v22, v23\n"
    "v_exp_f32 v25, v25\n"
    "v_exp_f32 v26, v26\n"
    "v_mfma_f32_32x32x16_bf16 v[208:223], v[56:59], v[96:99], v[208:223]\n"
    "v_exp_f32 v27, v27\n"
    "v_cvt_pk_bf16_f32 v100, v24, v25\n"
    "v_exp_f32 v28, v28\n"
    "v_cvt_pk_bf16_f32 v101, v26, v27\n"
    "v_mfma_f32_32x32x16_bf16 v[80:95], v[108:111], v[136:139], v[80:95]\n"
    "v_add_u32 v252, 0xe000, v252\n"
    "ds_read_b64_tr_b16 v[108:109], v252 offset:57344\n"
    "ds_read_b64_tr_b16 v[110:111], v252 offset:58368\n"
    "v_exp_f32 v29, v29\n"
    "v_exp_f32 v30, v30\n"
    "v_exp_f32 v31, v31\n"
    "v_cvt_pk_bf16_f32 v102, v28, v29\n"
    "v_cvt_pk_bf16_f32 v103, v30, v31\n"
    "s_waitcnt vmcnt(10) lgkmcnt(0)\n"
    "s_barrier\n"
    "s_setprio 3\n"
    "v_mfma_f32_32x32x16_bf16 v[16:31], v[32:35], v[120:123], 0\n"
    "v_exp_f32 v0, v0\n"
    "v_exp_f32 v1, v1\n"
    "v_exp_f32 v2, v2\n"
    "v_mfma_f32_32x32x16_bf16 v[208:223], v[60:63], v[100:103], v[208:223]\n"
    "s_xor_b32 s47, s63, 0x5000\n"
    "s_mov_b64 exec, s[36:37]\n"
    "buffer_load_dwordx4 v[56:59], v254, s[20:23], s47 offen sc1\n"
    "buffer_load_dwordx4 v[60:63], v254, s[20:23], s47 offen offset:2048 sc1\n"
    "s_mov_b64 exec, -1\n"
    "v_exp_f32 v3, v3\n"
    "v_cvt_pk_bf16_f32 v96, v0, v1\n"
    "v_exp_f32 v4, v4\n"
    "v_cvt_pk_bf16_f32 v97, v2, v3\n"
    "v_mfma_f32_32x32x16_bf16 v[16:31], v[36:39], v[124:127], v[16:31]\n"
    "s_add_u32 s64, s64, 0xffff4000\n"
    "s_xor_b32 s64, s64, 0x1000\n"
    "buffer_load_dwordx4 v[128:131], v244, s[16:19], s64 offen sc1\n"
    "v_exp_f32 v5, v5\n"
    "v_exp_f32 v6, v6\n"
    "v_exp_f32 v7, v7\n"
    "v_cvt_pk_bf16_f32 v98, v4, v5\n"
    "s_setprio 2\n"
    "s_waitcnt lgkmcnt(0)\n"
    "v_mfma_f32_32x32x16_bf16 v[64:79], v[104:107], v[140:143], v[64:79]\n"
    "ds_read_b64_tr_b16 v[104:105], v251 offset:24576\n"
    "ds_read_b64_tr_b16 v[106:107], v251 offset:25600\n"
    "v_exp_f32 v8, v8\n"
    "v_cvt_pk_bf16_f32 v99, v6, v7\n"
    "v_exp_f32 v9, v9\n"
    "v_exp_f32 v10, v10\n"
    "s_waitcnt vmcnt(11)\n"
    "v_mfma_f32_32x32x16_bf16 v[160:175], v[48:51], v[96:99], v[160:175]\n"
    "s_add_u32 m0, s50, 0x8000\n"
    "s_add_u32 s46, s60, 0xe0000\n"
    "buffer_load_dwordx4 v249, s[24:27], s46 offen lds\n"
    "v_exp_f32 v11, v11\n"
    "v_cvt_pk_bf16_f32 v100, v8, v9\n"
    "v_exp_f32 v12, v12\n"
    "v_cvt_pk_bf16_f32 v101, v10, v11\n"
    "v_mfma_f32_32x32x16_bf16 v[80:95], v[108:111], v[140:143], v[80:95]\n"
    "ds_read_b64_tr_b16 v[108:109], v252 offset:24576\n"
    "ds_read_b64_tr_b16 v[110:111], v252 offset:25600\n"
    "v_exp_f32 v13, v13\n"
    "v_exp_f32 v14, v14\n"
    "v_exp_f32 v15, v15\n"
    "v_cvt_pk_bf16_f32 v102, v12, v13\n"
    "v_cvt_pk_bf16_f32 v103, v14, v15\n"
    "s_setprio 1\n"
    "v_mfma_f32_32x32x16_bf16 v[0:15], v[32:35], v[224:227], 0\n"
    "v_exp_f32 v16, v16\n"
    "v_exp_f32 v17, v17\n"
    "v_exp_f32 v18, v18\n"
    "s_waitcnt vmcnt(11)\n"
    "v_mfma_f32_32x32x16_bf16 v[160:175], v[52:55], v[100:103], v[160:175]\n"
    "s_add_u32 s64, s64, 0x4000\n"
    "buffer_load_dwordx4 v[132:135], v244, s[16:19], s64 offen sc1\n"
    "v_exp_f32 v19, v19\n"
    "v_cvt_pk_bf16_f32 v96, v16, v17\n"
    "v_exp_f32 v20, v20\n"
    "v_cvt_pk_bf16_f32 v97, v18, v19\n"
    "v_mfma_f32_32x32x16_bf16 v[0:15], v[36:39], v[228:231], v[0:15]\n"
    "s_add_u32 m0, s50, 0xc000\n"
    "s_add_u32 s46, s60, 0x118000\n"
    "buffer_load_dwordx4 v249, s[24:27], s46 offen lds\n"
    "v_exp_f32 v21, v21\n"
    "v_exp_f32 v22, v22\n"
    "v_exp_f32 v23, v23\n"
    "v_cvt_pk_bf16_f32 v98, v20, v21\n"
    "s_setprio 0\n"
    "s_waitcnt lgkmcnt(0)\n"
    "s_waitcnt vmcnt(12)\n"
    "v_mfma_f32_32x32x16_bf16 v[64:79], v[104:107], v[144:147], v[64:79]\n"
    "ds_read_b64_tr_b16 v[104:105], v251 offset:32768\n"
    "ds_read_b64_tr_b16 v[106:107], v251 offset:33792\n"
    "v_exp_f32 v24, v24\n"
    "v_cvt_pk_bf16_f32 v99, v22, v23\n"
    "v_exp_f32 v25, v25\n"
    "v_exp_f32 v26, v26\n"
    "v_mfma_f32_32x32x16_bf16 v[176:191], v[48:51], v[96:99], v[176:191]\n"
    "s_add_u32 m0, s50, 0x10000\n"
    "s_add_u32 s46, s60, 0x150000\n"
    "buffer_load_dwordx4 v249, s[24:27], s46 offen lds\n"
    "v_exp_f32 v27, v27\n"
    "v_cvt_pk_bf16_f32 v100, v24, v25\n"
    "v_exp_f32 v28, v28\n"
    "v_cvt_pk_bf16_f32 v101, v26, v27\n"
    "v_mfma_f32_32x32x16_bf16 v[80:95], v[108:111], v[144:147], v[80:95]\n"
    "ds_read_b64_tr_b16 v[108:109], v252 offset:32768\n"
    "ds_read_b64_tr_b16 v[110:111], v252 offset:33792\n"
    "v_exp_f32 v29, v29\n"
    "v_exp_f32 v30, v30\n"
    "v_exp_f32 v31, v31\n"
    "v_cvt_pk_bf16_f32 v102, v28, v29\n"
    "v_cvt_pk_bf16_f32 v103, v30, v31\n"
    "s_setprio 3\n"
    "v_mfma_f32_32x32x16_bf16 v[16:31], v[32:35], v[232:235], 0\n"
    "v_exp_f32 v0, v0\n"
    "v_exp_f32 v1, v1\n"
    "v_exp_f32 v2, v2\n"
    "v_mfma_f32_32x32x16_bf16 v[176:191], v[52:55], v[100:103], v[176:191]\n"
    "s_xor_b32 s47, s61, 0xc00\n"
    "buffer_load_dwordx4 v[32:35], v245, s[20:23], s47 offen sc1\n"
    "v_exp_f32 v3, v3\n"
    "v_cvt_pk_bf16_f32 v96, v0, v1\n"
    "v_exp_f32 v4, v4\n"
    "v_cvt_pk_bf16_f32 v97, v2, v3\n"
    "v_mfma_f32_32x32x16_bf16 v[16:31], v[36:39], v[236:239], v[16:31]\n"
    "s_xor_b32 s47, s62, 0xc00\n"
    "buffer_load_dwordx4 v[36:39], v245, s[20:23], s47 offen sc1\n"
    "v_exp_f32 v5, v5\n"
    "v_exp_f32 v6, v6\n"
    "v_exp_f32 v7, v7\n"
    "v_cvt_pk_bf16_f32 v98, v4, v5\n"
    "s_setprio 2\n"
    "s_waitcnt lgkmcnt(0)\n"
    "s_waitcnt vmcnt(13)\n"
    "v_mfma_f32_32x32x16_bf16 v[64:79], v[104:107], v[148:151], v[64:79]\n"
    "ds_read_b64_tr_b16 v[104:105], v251 offset:40960\n"
    "ds_read_b64_tr_b16 v[106:107], v251 offset:41984\n"
    "v_exp_f32 v8, v8\n"
    "v_cvt_pk_bf16_f32 v99, v6, v7\n"
    "v_exp_f32 v9, v9\n"
    "v_exp_f32 v10, v10\n"
    "v_mfma_f32_32x32x16_bf16 v[192:207], v[48:51], v[96:99], v[192:207]\n"
    "s_add_u32 s64, s64, 0x4000\n"
    "buffer_load_dwordx4 v[136:139], v244, s[16:19], s64 offen sc1\n"
    "v_exp_f32 v11, v11\n"
    "v_cvt_pk_bf16_f32 v100, v8, v9\n"
    "v_exp_f32 v12, v12\n"
    "v_cvt_pk_bf16_f32 v101, v10, v11\n"
    "v_mfma_f32_32x32x16_bf16 v[80:95], v[108:111], v[148:151], v[80:95]\n"
    "ds_read_b64_tr_b16 v[108:109], v252 offset:40960\n"
    "ds_read_b64_tr_b16 v[110:111], v252 offset:41984\n"
    "v_exp_f32 v13, v13\n"
    "v_exp_f32 v14, v14\n"
    "v_exp_f32 v15, v15\n"
    "v_cvt_pk_bf16_f32 v102, v12, v13\n"
    "v_cvt_pk_bf16_f32 v103, v14, v15\n"
    "s_setprio 1\n"
    "s_waitcnt vmcnt(13)\n"
    "v_mfma_f32_32x32x16_bf16 v[0:15], v[40:43], v[112:115], 0\n"
    "v_exp_f32 v16, v16\n"
    "v_exp_f32 v17, v17\n"
    "v_exp_f32 v18, v18\n"
    "v_mfma_f32_32x32x16_bf16 v[192:207], v[52:55], v[100:103], v[192:207]\n"
    "s_add_u32 s64, s64, 0x4000\n"
    "buffer_load_dwordx4 v[140:143], v244, s[16:19], s64 offen sc1\n"
    "v_exp_f32 v19, v19\n"
    "v_cvt_pk_bf16_f32 v96, v16, v17\n"
    "v_exp_f32 v20, v20\n"
    "v_cvt_pk_bf16_f32 v97, v18, v19\n"
    "s_waitcnt vmcnt(13)\n"
    "v_mfma_f32_32x32x16_bf16 v[0:15], v[44:47], v[116:119], v[0:15]\n"
    "s_xor_b32 s60, s73, 3\n"
    "s_mul_i32 s60, s60, 0xe000\n"
    "s_add_u32 m0, s50, 0x22000\n"
    "s_add_u32 s46, s60, 0xa8000\n"
    "buffer_load_dwordx4 v249, s[24:27], s46 offen lds\n"
    "v_exp_f32 v21, v21\n"
    "v_exp_f32 v22, v22\n"
    "v_exp_f32 v23, v23\n"
    "v_cvt_pk_bf16_f32 v98, v20, v21\n"
    "s_setprio 0\n"
    "s_waitcnt lgkmcnt(0)\n"
    "s_waitcnt vmcnt(13)\n"
    "v_mfma_f32_32x32x16_bf16 v[64:79], v[104:107], v[152:155], v[64:79]\n"
    "ds_read_b64_tr_b16 v[104:105], v251 offset:49152\n"
    "ds_read_b64_tr_b16 v[106:107], v251 offset:50176\n"
    "v_exp_f32 v24, v24\n"
    "v_cvt_pk_bf16_f32 v99, v22, v23\n"
    "v_exp_f32 v25, v25\n"
    "v_exp_f32 v26, v26\n"
    "v_mfma_f32_32x32x16_bf16 v[208:223], v[48:51], v[96:99], v[208:223]\n"
    "s_add_u32 m0, s50, 0x24000\n"
    "s_add_u32 s46, s60, 0xe0000\n"
    "buffer_load_dwordx4 v249, s[24:27], s46 offen lds\n"
    "v_exp_f32 v27, v27\n"
    "v_cvt_pk_bf16_f32 v100, v24, v25\n"
    "v_exp_f32 v28, v28\n"
    "v_cvt_pk_bf16_f32 v101, v26, v27\n"
    "v_mfma_f32_32x32x16_bf16 v[80:95], v[108:111], v[152:155], v[80:95]\n"
    "ds_read_b64_tr_b16 v[108:109], v252 offset:49152\n"
    "ds_read_b64_tr_b16 v[110:111], v252 offset:50176\n"
    "v_exp_f32 v29, v29\n"
    "v_exp_f32 v30, v30\n"
    "v_exp_f32 v31, v31\n"
    "v_cvt_pk_bf16_f32 v102, v28, v29\n"
    "v_cvt_pk_bf16_f32 v103, v30, v31\n"
    "s_setprio 3\n"
    "v_mfma_f32_32x32x16_bf16 v[16:31], v[40:43], v[120:123], 0\n"
    "v_exp_f32 v0, v0\n"
    "v_exp_f32 v1, v1\n"
    "v_exp_f32 v2, v2\n"
    "v_mfma_f32_32x32x16_bf16 v[208:223], v[52:55], v[100:103], v[208:223]\n"
    "s_xor_b32 s47, s63, 0x6000\n"
    "s_mov_b64 exec, s[36:37]\n"
    "buffer_load_dwordx4 v[48:51], v254, s[20:23], s47 offen sc1\n"
    "buffer_load_dwordx4 v[52:55], v254, s[20:23], s47 offen offset:2048 sc1\n"
    "s_mov_b64 exec, -1\n"
    "v_exp_f32 v3, v3\n"
    "v_cvt_pk_bf16_f32 v96, v0, v1\n"
    "v_exp_f32 v4, v4\n"
    "v_cvt_pk_bf16_f32 v97, v2, v3\n"
    "v_mfma_f32_32x32x16_bf16 v[16:31], v[44:47], v[124:127], v[16:31]\n"
    "s_add_u32 s64, s64, 0xffff4000\n"
    "s_xor_b32 s64, s64, 0x3000\n"
    "buffer_load_dwordx4 v[144:147], v244, s[16:19], s64 offen sc1\n"
    "v_exp_f32 v5, v5\n"
    "v_exp_f32 v6, v6\n"
    "v_exp_f32 v7, v7\n"
    "v_cvt_pk_bf16_f32 v98, v4, v5\n"
    "s_setprio 2\n"
    "s_waitcnt lgkmcnt(0)\n"
    "s_waitcnt vmcnt(16)\n"
    "v_mfma_f32_32x32x16_bf16 v[64:79], v[104:107], v[156:159], v[64:79]\n"
    "ds_read_b64_tr_b16 v[104:105], v251 offset:8192\n"
    "ds_read_b64_tr_b16 v[106:107], v251 offset:9216\n"
    "v_exp_f32 v8, v8\n"
    "v_cvt_pk_bf16_f32 v99, v6, v7\n"
    "v_exp_f32 v9, v9\n"
    "v_exp_f32 v10, v10\n"
    "s_waitcnt vmcnt(15)\n"
    "v_mfma_f32_32x32x16_bf16 v[160:175], v[56:59], v[96:99], v[160:175]\n"
    "s_add_u32 m0, s50, 0x20000\n"
    "s_add_u32 s46, s60, 0x118000\n"
    "buffer_load_dwordx4 v249, s[24:27], s46 offen lds\n"
    "v_exp_f32 v11, v11\n"
    "v_cvt_pk_bf16_f32 v100, v8, v9\n"
    "v_exp_f32 v12, v12\n"
    "v_cvt_pk_bf16_f32 v101, v10, v11\n"
    "v_mfma_f32_32x32x16_bf16 v[80:95], v[108:111], v[156:159], v[80:95]\n"
    "ds_read_b64_tr_b16 v[108:109], v252 offset:8192\n"
    "ds_read_b64_tr_b16 v[110:111], v252 offset:9216\n"
    "v_exp_f32 v13, v13\n"
    "v_exp_f32 v14, v14\n"
    "v_exp_f32 v15, v15\n"
    "v_cvt_pk_bf16_f32 v102, v12, v13\n"
    "v_cvt_pk_bf16_f32 v103, v14, v15\n"
    "s_setprio 1\n"
    "v_mfma_f32_32x32x16_bf16 v[0:15], v[40:43], v[224:227], 0\n"
    "v_exp_f32 v16, v16\n"
    "v_exp_f32 v17, v17\n"
    "v_exp_f32 v18, v18\n"
    "s_waitcnt vmcnt(15)\n"
    "v_mfma_f32_32x32x16_bf16 v[160:175], v[60:63], v[100:103], v[160:175]\n"
    "s_add_u32 s64, s64, 0x4000\n"
    "buffer_load_dwordx4 v[148:151], v244, s[16:19], s64 offen sc1\n"
    "v_exp_f32 v19, v19\n"
    "v_cvt_pk_bf16_f32 v96, v16, v17\n"
    "v_exp_f32 v20, v20\n"
    "v_cvt_pk_bf16_f32 v97, v18, v19\n"
    "v_mfma_f32_32x32x16_bf16 v[0:15], v[44:47], v[228:231], v[0:15]\n"
    "s_add_u32 m0, s50, 0x26000\n"
    "s_add_u32 s46, s60, 0x150000\n"
    "buffer_load_dwordx4 v249, s[24:27], s46 offen lds\n"
    "v_exp_f32 v21, v21\n"
    "v_exp_f32 v22, v22\n"
    "v_exp_f32 v23, v23\n"
    "v_cvt_pk_bf16_f32 v98, v20, v21\n"
    "s_setprio 0\n"
    "s_waitcnt lgkmcnt(0)\n"
    "s_waitcnt vmcnt(16)\n"
    "v_mfma_f32_32x32x16_bf16 v[64:79], v[104:107], v[128:131], v[64:79]\n"
    "ds_read_b64_tr_b16 v[104:105], v251 offset:16384\n"
    "ds_read_b64_tr_b16 v[106:107], v251 offset:17408\n"
    "v_exp_f32 v24, v24\n"
    "v_cvt_pk_bf16_f32 v99, v22, v23\n"
    "v_exp_f32 v25, v25\n"
    "v_exp_f32 v26, v26\n"
    "v_mfma_f32_32x32x16_bf16 v[176:191], v[56:59], v[96:99], v[176:191]\n"
    "v_exp_f32 v27, v27\n"
    "v_cvt_pk_bf16_f32 v100, v24, v25\n"
    "v_exp_f32 v28, v28\n"
    "v_cvt_pk_bf16_f32 v101, v26, v27\n"
    "v_mfma_f32_32x32x16_bf16 v[80:95], v[108:111], v[128:131], v[80:95]\n"
    "ds_read_b64_tr_b16 v[108:109], v252 offset:16384\n"
    "ds_read_b64_tr_b16 v[110:111], v252 offset:17408\n"
    "v_exp_f32 v29, v29\n"
    "v_exp_f32 v30, v30\n"
    "v_exp_f32 v31, v31\n"
    "v_cvt_pk_bf16_f32 v102, v28, v29\n"
    "v_cvt_pk_bf16_f32 v103, v30, v31\n"
    "s_setprio 3\n"
    "v_mfma_f32_32x32x16_bf16 v[16:31], v[40:43], v[232:235], 0\n"
    "v_exp_f32 v0, v0\n"
    "v_exp_f32 v1, v1\n"
    "v_exp_f32 v2, v2\n"
    "v_mfma_f32_32x32x16_bf16 v[176:191], v[60:63], v[100:103], v[176:191]\n"
    "s_xor_b32 s47, s61, 0xe00\n"
    "buffer_load_dwordx4 v[40:43], v245, s[20:23], s47 offen sc1\n"
    "v_exp_f32 v3, v3\n"
    "v_cvt_pk_bf16_f32 v96, v0, v1\n"
    "v_exp_f32 v4, v4\n"
    "v_cvt_pk_bf16_f32 v97, v2, v3\n"
    "v_mfma_f32_32x32x16_bf16 v[16:31], v[44:47], v[236:239], v[16:31]\n"
    "s_xor_b32 s47, s62, 0xe00\n"
    "buffer_load_dwordx4 v[44:47], v245, s[20:23], s47 offen sc1\n"
    "v_exp_f32 v5, v5\n"
    "v_exp_f32 v6, v6\n"
    "v_exp_f32 v7, v7\n"
    "v_cvt_pk_bf16_f32 v98, v4, v5\n"
    "s_setprio 2\n"
    "s_waitcnt lgkmcnt(0)\n"
    "s_waitcnt vmcnt(16)\n"
    "v_mfma_f32_32x32x16_bf16 v[64:79], v[104:107], v[132:135], v[64:79]\n"
    "v_add_u32 v251, 0xffff0000, v251\n"
    "ds_read_b64_tr_b16 v[104:105], v251\n"
    "ds_read_b64_tr_b16 v[106:107], v251 offset:1024\n"
    "v_exp_f32 v8, v8\n"
    "v_cvt_pk_bf16_f32 v99, v6, v7\n"
    "v_exp_f32 v9, v9\n"
    "v_exp_f32 v10, v10\n"
    "v_mfma_f32_32x32x16_bf16 v[192:207], v[56:59], v[96:99], v[192:207]\n"
    "s_add_u32 s64, s64, 0x4000\n"
    "buffer_load_dwordx4 v[152:155], v244, s[16:19], s64 offen sc1\n"
    "v_exp_f32 v11, v11\n"
    "v_cvt_pk_bf16_f32 v100, v8, v9\n"
    "v_exp_f32 v12, v12\n"
    "v_cvt_pk_bf16_f32 v101, v10, v11\n"
    "v_mfma_f32_32x32x16_bf16 v[80:95], v[108:111], v[132:135], v[80:95]\n"
    "v_add_u32 v252, 0xffff0000, v252\n"
    "ds_read_b64_tr_b16 v[108:109], v252\n"
    "ds_read_b64_tr_b16 v[110:111], v252 offset:1024\n"
    "v_exp_f32 v13, v13\n"
    "v_exp_f32 v14, v14\n"
    "v_exp_f32 v15, v15\n"
    "v_cvt_pk_bf16_f32 v102, v12, v13\n"
    "v_cvt_pk_bf16_f32 v103, v14, v15\n"
    "s_waitcnt vmcnt(16) lgkmcnt(0)\n"
    "s_barrier\n"
    "s_setprio 1\n"
    "s_waitcnt vmcnt(14)\n"
    "v_mfma_f32_32x32x16_bf16 v[0:15], v[32:35], v[112:115], 0\n"
    "v_exp_f32 v16, v16\n"
    "v_exp_f32 v17, v17\n"
    "v_exp_f32 v18, v18\n"
    "v_mfma_f32_32x32x16_bf16 v[192:207], v[60:63], v[100:103], v[192:207]\n"
    "s_add_u32 s64, s64, 0x4000\n"
    "buffer_load_dwordx4 v[156:159], v244, s[16:19], s64 offen sc1\n"
    "v_exp_f32 v19, v19\n"
    "v_cvt_pk_bf16_f32 v96, v16, v17\n"
    "v_exp_f32 v20, v20\n"
    "v_cvt_pk_bf16_f32 v97, v18, v19\n"
    "s_waitcnt vmcnt(14)\n"
    "v_mfma_f32_32x32x16_bf16 v[0:15], v[36:39], v[116:119], v[0:15]\n"
    "s_add_u32 s64, s64, 0xffff4000\n"
    "s_xor_b32 s64, s64, 0x1000\n"
    "buffer_load_dwordx4 v[128:131], v244, s[16:19], s64 offen sc1\n"
    "v_exp_f32 v21, v21\n"
    "v_exp_f32 v22, v22\n"
    "v_exp_f32 v23, v23\n"
    "v_cvt_pk_bf16_f32 v98, v20, v21\n"
    "s_setprio 0\n"
    "s_waitcnt lgkmcnt(0)\n"
    "s_waitcnt vmcnt(14)\n"
    "v_mfma_f32_32x32x16_bf16 v[64:79], v[104:107], v[136:139], v[64:79]\n"
    "ds_read_b64_tr_b16 v[104:105], v251 offset:49152\n"
    "ds_read_b64_tr_b16 v[106:107], v251 offset:50176\n"
    "v_exp_f32 v24, v24\n"
    "v_cvt_pk_bf16_f32 v99, v22, v23\n"
    "v_exp_f32 v25, v25\n"
    "v_exp_f32 v26, v26\n"
    "v_mfma_f32_32x32x16_bf16 v[208:223], v[56:59], v[96:99], v[208:223]\n"
    "s_add_u32 s64, s64, 0x4000\n"
    "buffer_load_dwordx4 v[132:135], v244, s[16:19], s64 offen sc1\n"
    "v_exp_f32 v27, v27\n"
    "v_cvt_pk_bf16_f32 v100, v24, v25\n"
    "v_exp_f32 v28, v28\n"
    "v_cvt_pk_bf16_f32 v101, v26, v27\n"
    "v_mfma_f32_32x32x16_bf16 v[80:95], v[108:111], v[136:139], v[80:95]\n"
    "ds_read_b64_tr_b16 v[108:109], v252 offset:49152\n"
    "ds_read_b64_tr_b16 v[110:111], v252 offset:50176\n"
    "v_exp_f32 v29, v29\n"
    "v_exp_f32 v30, v30\n"
    "v_exp_f32 v31, v31\n"
    "v_cvt_pk_bf16_f32 v102, v28, v29\n"
    "v_cvt_pk_bf16_f32 v103, v30, v31\n"
    "s_setprio 3\n"
    "v_mfma_f32_32x32x16_bf16 v[16:31], v[32:35], v[120:123], 0\n"
    "v_exp_f32 v0, v0\n"
    "v_exp_f32 v1, v1\n"
    "v_exp_f32 v2, v2\n"
    "v_mfma_f32_32x32x16_bf16 v[208:223], v[60:63], v[100:103], v[208:223]\n"
    "s_xor_b32 s47, s63, 0x7000\n"
    "s_mov_b64 exec, s[36:37]\n"
    "buffer_load_dwordx4 v[56:59], v254, s[20:23], s47 offen sc1\n"
    "buffer_load_dwordx4 v[60:63], v254, s[20:23], s47 offen offset:2048 sc1\n"
    "s_mov_b64 exec, -1\n"
    "v_exp_f32 v3, v3\n"
    "v_cvt_pk_bf16_f32 v96, v0, v1\n"
    "v_exp_f32 v4, v4\n"
    "v_cvt_pk_bf16_f32 v97, v2, v3\n"
    "v_mfma_f32_32x32x16_bf16 v[16:31], v[36:39], v[124:127], v[16:31]\n"
    "s_add_u32 s64, s64, 0x4000\n"
    "buffer_load_dwordx4 v[136:139], v244, s[16:19], s64 offen sc1\n"
    "v_exp_f32 v5, v5\n"
    "v_exp_f32 v6, v6\n"
    "v_exp_f32 v7, v7\n"
    "v_cvt_pk_bf16_f32 v98, v4, v5\n"
    "s_setprio 2\n"
    "s_waitcnt lgkmcnt(0)\n"
    "s_waitcnt vmcnt(17)\n"
    "v_mfma_f32_32x32x16_bf16 v[64:79], v[104:107], v[140:143], v[64:79]\n"
    "ds_read_b64_tr_b16 v[104:105], v251 offset:32768\n"
    "ds_read_b64_tr_b16 v[106:107], v251 offset:33792\n"
    "v_exp_f32 v8, v8\n"
    "v_cvt_pk_bf16_f32 v99, v6, v7\n"
    "v_exp_f32 v9, v9\n"
    "v_exp_f32 v10, v10\n"
    "s_waitcnt vmcnt(14)\n"
    "v_mfma_f32_32x32x16_bf16 v[160:175], v[48:51], v[96:99], v[160:175]\n"
    "s_add_u32 m0, s50, 0x12000\n"
    "s_mov_b32 s46, 0\n"
    "buffer_load_dwordx4 v249, s[28:31], s46 offen lds\n"
    "v_exp_f32 v11, v11\n"
    "v_cvt_pk_bf16_f32 v100, v8, v9\n"
    "v_exp_f32 v12, v12\n"
    "v_cvt_pk_bf16_f32 v101, v10, v11\n"
    "v_mfma_f32_32x32x16_bf16 v[80:95], v[108:111], v[140:143], v[80:95]\n"
    "ds_read_b64_tr_b16 v[108:109], v252 offset:32768\n"
    "ds_read_b64_tr_b16 v[110:111], v252 offset:33792\n"
    "v_exp_f32 v13, v13\n"
    "v_exp_f32 v14, v14\n"
    "v_exp_f32 v15, v15\n"
    "v_cvt_pk_bf16_f32 v102, v12, v13\n"
    "v_cvt_pk_bf16_f32 v103, v14, v15\n"
    "s_setprio 1\n"
    "s_waitcnt vmcnt(9)\n"
    "v_mfma_f32_32x32x16_bf16 v[0:15], v[40:43], v[112:115], 0\n"
    "v_bfe_u32 v246, v244, 4, 5\n"
    "v_lshrrev_b32 v247, 9, v244\n"
    "v_mul_u32_u24 v253, 0x380, v246\n"
    "v_lshl_add_u32 v253, v247, 4, v253\n"
    "v_exp_f32 v16, v16\n"
    "v_exp_f32 v17, v17\n"
    "v_exp_f32 v18, v18\n"
    "v_mfma_f32_32x32x16_bf16 v[160:175], v[52:55], v[100:103], v[160:175]\n"
    "s_add_u32 s64, s64, 0x4000\n"
    "buffer_load_dwordx4 v[140:143], v244, s[16:19], s64 offen sc1\n"
    "v_exp_f32 v19, v19\n"
    "v_cvt_pk_bf16_f32 v96, v16, v17\n"
    "v_exp_f32 v20, v20\n"
    "v_cvt_pk_bf16_f32 v97, v18, v19\n"
    "s_waitcnt vmcnt(9)\n"
    "v_mfma_f32_32x32x16_bf16 v[0:15], v[44:47], v[116:119], v[0:15]\n"
    "s_add_u32 m0, s50, 0x14000\n"
    "s_mov_b32 s46, 0xe000\n"
    "buffer_load_dwordx4 v249, s[28:31], s46 offen lds\n"
    "v_exp_f32 v21, v21\n"
    "v_exp_f32 v22, v22\n"
    "v_exp_f32 v23, v23\n"
    "v_cvt_pk_bf16_f32 v98, v20, v21\n"
    "s_setprio 0\n"
    "s_waitcnt lgkmcnt(0)\n"
    "v_mfma_f32_32x32x16_bf16 v[64:79], v[104:107], v[144:147], v[64:79]\n"
    "ds_read_b64_tr_b16 v[104:105], v251 offset:24576\n"
    "ds_read_b64_tr_b16 v[106:107], v251 offset:25600\n"
    "v_exp_f32 v24, v24\n"
    "v_cvt_pk_bf16_f32 v99, v22, v23\n"
    "v_exp_f32 v25, v25\n"
    "v_exp_f32 v26, v26\n"
    "v_mfma_f32_32x32x16_bf16 v[176:191], v[48:51], v[96:99], v[176:191]\n"
    "s_add_u32 m0, s50, 0x16000\n"
    "s_mov_b32 s46, 0x1c000\n"
    "buffer_load_dwordx4 v249, s[28:31], s46 offen lds\n"
    "v_exp_f32 v27, v27\n"
    "v_cvt_pk_bf16_f32 v100, v24, v25\n"
    "v_exp_f32 v28, v28\n"
    "v_cvt_pk_bf16_f32 v101, v26, v27\n"
    "v_mfma_f32_32x32x16_bf16 v[80:95], v[108:111], v[144:147], v[80:95]\n"
    "ds_read_b64_tr_b16 v[108:109], v252 offset:24576\n"
    "ds_read_b64_tr_b16 v[110:111], v252 offset:25600\n"
    "v_exp_f32 v29, v29\n"
    "v_exp_f32 v30, v30\n"
    "v_exp_f32 v31, v31\n"
    "v_cvt_pk_bf16_f32 v102, v28, v29\n"
    "v_cvt_pk_bf16_f32 v103, v30, v31\n"
    "s_setprio 3\n"
    "v_mfma_f32_32x32x16_bf16 v[16:31], v[40:43], v[120:123], 0\n"
    "v_exp_f32 v0, v0\n"
    "v_exp_f32 v1, v1\n"
    "v_exp_f32 v2, v2\n"
    "v_mfma_f32_32x32x16_bf16 v[176:191], v[52:55], v[100:103], v[176:191]\n"
    "s_add_u32 m0, s50, 0x18000\n"
    "s_mov_b32 s46, 0x2a000\n"
    "buffer_load_dwordx4 v249, s[28:31], s46 offen lds\n"
    "v_exp_f32 v3, v3\n"
    "v_cvt_pk_bf16_f32 v96, v0, v1\n"
    "v_exp_f32 v4, v4\n"
    "v_cvt_pk_bf16_f32 v97, v2, v3\n"
    "v_mfma_f32_32x32x16_bf16 v[16:31], v[44:47], v[124:127], v[16:31]\n"
    "s_add_u32 m0, s50, 0x1a000\n"
    "s_mov_b32 s46, 0x38000\n"
    "buffer_load_dwordx4 v249, s[28:31], s46 offen lds\n"
    "v_exp_f32 v5, v5\n"
    "v_exp_f32 v6, v6\n"
    "v_exp_f32 v7, v7\n"
    "v_cvt_pk_bf16_f32 v98, v4, v5\n"
    "s_setprio 2\n"
    "s_waitcnt lgkmcnt(0)\n"
    "v_mfma_f32_32x32x16_bf16 v[64:79], v[104:107], v[148:151], v[64:79]\n"
    "ds_read_b64_tr_b16 v[104:105], v251 offset:40960\n"
    "ds_read_b64_tr_b16 v[106:107], v251 offset:41984\n"
    "v_exp_f32 v8, v8\n"
    "v_cvt_pk_bf16_f32 v99, v6, v7\n"
    "v_exp_f32 v9, v9\n"
    "v_exp_f32 v10, v10\n"
    "s_waitcnt vmcnt(8)\n"
    "v_mfma_f32_32x32x16_bf16 v[160:175], v[56:59], v[96:99], v[160:175]\n"
    "s_add_u32 m0, s50, 0x1c000\n"
    "s_mov_b32 s46, 0x46000\n"
    "buffer_load_dwordx4 v249, s[28:31], s46 offen lds\n"
    "v_exp_f32 v11, v11\n"
    "v_cvt_pk_bf16_f32 v100, v8, v9\n"
    "v_exp_f32 v12, v12\n"
    "v_cvt_pk_bf16_f32 v101, v10, v11\n"
    "v_mfma_f32_32x32x16_bf16 v[80:95], v[108:111], v[148:151], v[80:95]\n"
    "ds_read_b64_tr_b16 v[108:109], v252 offset:40960\n"
    "ds_read_b64_tr_b16 v[110:111], v252 offset:41984\n"
    "v_exp_f32 v13, v13\n"
    "v_exp_f32 v14, v14\n"
    "v_exp_f32 v15, v15\n"
    "v_cvt_pk_bf16_f32 v102, v12, v13\n"
    "v_cvt_pk_bf16_f32 v103, v14, v15\n"
    "s_setprio 1\n"
    "v_mfma_f32_32x32x16_bf16 v[0:15], v[32:35], v[224:227], 0\n"
    "v_exp_f32 v16, v16\n"
    "v_exp_f32 v17, v17\n"
    "v_exp_f32 v18, v18\n"
    "s_waitcnt vmcnt(8)\n"
    "v_mfma_f32_32x32x16_bf16 v[160:175], v[60:63], v[100:103], v[160:175]\n"
    "s_add_u32 m0, s50, 0x1e000\n"
    "s_mov_b32 s46, 0x54000\n"
    "buffer_load_dwordx4 v249, s[28:31], s46 offen lds\n"
    "v_exp_f32 v19, v19\n"
    "v_cvt_pk_bf16_f32 v96, v16, v17\n"
    "v_exp_f32 v20, v20\n"
    "v_cvt_pk_bf16_f32 v97, v18, v19\n"
    "v_mfma_f32_32x32x16_bf16 v[0:15], v[36:39], v[228:231], v[0:15]\n"
    "v_exp_f32 v21, v21\n"
    "v_exp_f32 v22, v22\n"
    "v_exp_f32 v23, v23\n"
    "v_cvt_pk_bf16_f32 v98, v20, v21\n"
    "s_setprio 0\n"
    "s_waitcnt lgkmcnt(0)\n"
    "v_mfma_f32_32x32x16_bf16 v[64:79], v[104:107], v[152:155], v[64:79]\n"
    "v_exp_f32 v24, v24\n"
    "v_cvt_pk_bf16_f32 v99, v22, v23\n"
    "v_exp_f32 v25, v25\n"
    "v_exp_f32 v26, v26\n"
    "v_mfma_f32_32x32x16_bf16 v[176:191], v[56:59], v[96:99], v[176:191]\n"
    "v_exp_f32 v27, v27\n"
    "v_cvt_pk_bf16_f32 v100, v24, v25\n"
    "v_exp_f32 v28, v28\n"
    "v_cvt_pk_bf16_f32 v101, v26, v27\n"
    "v_mfma_f32_32x32x16_bf16 v[80:95], v[108:111], v[152:155], v[80:95]\n"
    "v_xor_b32 v246, 0x800, v255\n"
    "ds_write_b128 v246, v[160:163]\n"
    "v_exp_f32 v29, v29\n"
    "v_exp_f32 v30, v30\n"
    "v_exp_f32 v31, v31\n"
    "v_cvt_pk_bf16_f32 v102, v28, v29\n"
    "v_cvt_pk_bf16_f32 v103, v30, v31\n"
    "s_barrier\n"
    "s_setprio 3\n"
    "v_mfma_f32_32x32x16_bf16 v[16:31], v[40:43], v[224:227], 0\n"
    "ds_write_b128 v246, v[164:167] offset:1024\n"
    "v_exp_f32 v0, v0\n"
    "v_exp_f32 v1, v1\n"
    "v_exp_f32 v2, v2\n"
    "v_mfma_f32_32x32x16_bf16 v[176:191], v[60:63], v[100:103], v[176:191]\n"
    "v_xor_b32 v247, 0x2800, v255\n"
    "ds_write_b128 v247, v[168:171] offset:16384\n"
    "v_exp_f32 v3, v3\n"
    "v_cvt_pk_bf16_f32 v96, v0, v1\n"
    "v_exp_f32 v4, v4\n"
    "v_cvt_pk_bf16_f32 v97, v2, v3\n"
    "v_mfma_f32_32x32x16_bf16 v[16:31], v[44:47], v[228:231], v[16:31]\n"
    "buffer_load_dwordx4 v[152:155], v253, s[4:7], s69 offen\n"
    "v_exp_f32 v5, v5\n"
    "v_exp_f32 v6, v6\n"
    "v_exp_f32 v7, v7\n"
    "v_cvt_pk_bf16_f32 v98, v4, v5\n"
    "s_setprio 2\n"
    "ds_write_b128 v247, v[172:175] offset:17408\n"
    "v_exp_f32 v8, v8\n"
    "v_cvt_pk_bf16_f32 v99, v6, v7\n"
    "v_exp_f32 v9, v9\n"
    "v_exp_f32 v10, v10\n"
    "v_mfma_f32_32x32x16_bf16 v[192:207], v[48:51], v[96:99], v[192:207]\n"
    "v_exp_f32 v11, v11\n"
    "v_cvt_pk_bf16_f32 v100, v8, v9\n"
    "v_exp_f32 v12, v12\n"
    "v_cvt_pk_bf16_f32 v101, v10, v11\n"
    "v_xor_b32 v246, 0x1000, v255\n"
    "ds_write_b128 v246, v[176:179] offset:32768\n"
    "v_exp_f32 v13, v13\n"
    "v_exp_f32 v14, v14\n"
    "v_exp_f32 v15, v15\n"
    "v_cvt_pk_bf16_f32 v102, v12, v13\n"
    "v_cvt_pk_bf16_f32 v103, v14, v15\n"
    "s_setprio 1\n"
    "v_mfma_f32_32x32x16_bf16 v[0:15], v[32:35], v[232:235], 0\n"
    "ds_write_b128 v246, v[180:183] offset:33792\n"
    "v_exp_f32 v16, v16\n"
    "v_exp_f32 v17, v17\n"
    "v_exp_f32 v18, v18\n"
    "v_mfma_f32_32x32x16_bf16 v[192:207], v[52:55], v[100:103], v[192:207]\n"
    "v_xor_b32 v247, 0x3000, v255\n"
    "ds_write_b128 v247, v[184:187] offset:49152\n"
    "v_exp_f32 v19, v19\n"
    "v_cvt_pk_bf16_f32 v96, v16, v17\n"
    "v_exp_f32 v20, v20\n"
    "v_cvt_pk_bf16_f32 v97, v18, v19\n"
    "v_mfma_f32_32x32x16_bf16 v[0:15], v[36:39], v[236:239], v[0:15]\n"
    "v_exp_f32 v21, v21\n"
    "v_exp_f32 v22, v22\n"
    "v_exp_f32 v23, v23\n"
    "v_cvt_pk_bf16_f32 v98, v20, v21\n"
    "s_setprio 0\n"
    "ds_write_b128 v247, v[188:191] offset:50176\n"
    "v_exp_f32 v24, v24\n"
    "v_cvt_pk_bf16_f32 v99, v22, v23\n"
    "v_exp_f32 v25, v25\n"
    "v_exp_f32 v26, v26\n"
    "v_mfma_f32_32x32x16_bf16 v[192:207], v[56:59], v[96:99], v[192:207]\n"
    "v_exp_f32 v27, v27\n"
    "v_cvt_pk_bf16_f32 v100, v24, v25\n"
    "v_exp_f32 v28, v28\n"
    "v_cvt_pk_bf16_f32 v101, v26, v27\n"
    "v_exp_f32 v29, v29\n"
    "v_exp_f32 v30, v30\n"
    "v_exp_f32 v31, v31\n"
    "v_cvt_pk_bf16_f32 v102, v28, v29\n"
    "v_cvt_pk_bf16_f32 v103, v30, v31\n"
    "s_waitcnt lgkmcnt(0)\n"
    "s_barrier\n"
    "s_setprio 3\n"
    "v_mfma_f32_32x32x16_bf16 v[16:31], v[40:43], v[232:235], 0\n"
    "ds_read_b128 v[160:163], v255\n"
    "ds_read_b128 v[164:167], v255 offset:1024\n"
    "ds_read_b128 v[168:171], v255 offset:16384\n"
    "ds_read_b128 v[172:175], v255 offset:17408\n"
    "v_exp_f32 v0, v0\n"
    "v_exp_f32 v1, v1\n"
    "v_exp_f32 v2, v2\n"
    "v_mfma_f32_32x32x16_bf16 v[192:207], v[60:63], v[100:103], v[192:207]\n"
    "ds_read_b128 v[176:179], v255 offset:32768\n"
    "ds_read_b128 v[180:183], v255 offset:33792\n"
    "ds_read_b128 v[184:187], v255 offset:49152\n"
    "ds_read_b128 v[188:191], v255 offset:50176\n"
    "v_exp_f32 v3, v3\n"
    "v_cvt_pk_bf16_f32 v96, v0, v1\n"
    "v_exp_f32 v4, v4\n"
    "v_cvt_pk_bf16_f32 v97, v2, v3\n"
    "v_mfma_f32_32x32x16_bf16 v[16:31], v[44:47], v[236:239], v[16:31]\n"
    "v_exp_f32 v5, v5\n"
    "v_exp_f32 v6, v6\n"
    "v_exp_f32 v7, v7\n"
    "v_cvt_pk_bf16_f32 v98, v4, v5\n"
    "s_setprio 2\n"
    "v_exp_f32 v8, v8\n"
    "v_cvt_pk_bf16_f32 v99, v6, v7\n"
    "v_exp_f32 v9, v9\n"
    "v_exp_f32 v10, v10\n"
    "s_waitcnt lgkmcnt(5)\n"
    "v_pk_add_f32 v[160:161], v[160:161], v[168:169]\n"
    "v_pk_add_f32 v[162:163], v[162:163], v[170:171]\n"
    "s_waitcnt lgkmcnt(4)\n"
    "v_pk_add_f32 v[164:165], v[164:165], v[172:173]\n"
    "v_pk_add_f32 v[166:167], v[166:167], v[174:175]\n"
    "v_mfma_f32_32x32x16_bf16 v[208:223], v[48:51], v[96:99], v[208:223]\n"
    "v_exp_f32 v11, v11\n"
    "v_cvt_pk_bf16_f32 v100, v8, v9\n"
    "v_exp_f32 v12, v12\n"
    "v_cvt_pk_bf16_f32 v101, v10, v11\n"
    "ds_write_b128 v255, v[192:195]\n"
    "v_exp_f32 v13, v13\n"
    "v_exp_f32 v14, v14\n"
    "v_exp_f32 v15, v15\n"
    "v_cvt_pk_bf16_f32 v102, v12, v13\n"
    "v_cvt_pk_bf16_f32 v103, v14, v15\n"
    "s_waitcnt lgkmcnt(2)\n"
    "v_pk_add_f32 v[176:177], v[176:177], v[184:185]\n"
    "v_pk_add_f32 v[178:179], v[178:179], v[186:187]\n"
    "s_waitcnt lgkmcnt(1)\n"
    "v_pk_add_f32 v[180:181], v[180:181], v[188:189]\n"
    "v_pk_add_f32 v[182:183], v[182:183], v[190:191]\n"
    "s_setprio 1\n"
    "ds_write_b128 v255, v[196:199] offset:1024\n"
    "v_exp_f32 v16, v16\n"
    "v_exp_f32 v17, v17\n"
    "v_exp_f32 v18, v18\n"
    "v_mfma_f32_32x32x16_bf16 v[208:223], v[52:55], v[100:103], v[208:223]\n"
    "v_exp_f32 v19, v19\n"
    "v_cvt_pk_bf16_f32 v96, v16, v17\n"
    "v_exp_f32 v20, v20\n"
    "v_cvt_pk_bf16_f32 v97, v18, v19\n"
    "v_pk_add_f32 v[160:161], v[160:161], v[176:177]\n"
    "v_pk_add_f32 v[162:163], v[162:163], v[178:179]\n"
    "v_pk_add_f32 v[164:165], v[164:165], v[180:181]\n"
    "v_pk_add_f32 v[166:167], v[166:167], v[182:183]\n"
    "ds_write_b128 v255, v[200:203] offset:16384\n"
    "v_exp_f32 v21, v21\n"
    "v_exp_f32 v22, v22\n"
    "v_exp_f32 v23, v23\n"
    "v_cvt_pk_bf16_f32 v98, v20, v21\n"
    "s_setprio 0\n"
    "ds_read_b64_tr_b16 v[104:105], v252 offset:57344\n"
    "ds_read_b64_tr_b16 v[106:107], v252 offset:58368\n"
    "v_exp_f32 v24, v24\n"
    "v_cvt_pk_bf16_f32 v99, v22, v23\n"
    "v_exp_f32 v25, v25\n"
    "v_exp_f32 v26, v26\n"
    "v_mfma_f32_32x32x16_bf16 v[208:223], v[56:59], v[96:99], v[208:223]\n"
    "ds_write_b128 v255, v[204:207] offset:17408\n"
    "v_exp_f32 v27, v27\n"
    "v_cvt_pk_bf16_f32 v100, v24, v25\n"
    "v_exp_f32 v28, v28\n"
    "v_cvt_pk_bf16_f32 v101, v26, v27\n"
    "v_add_u32 v252, 0x16000, v252\n"
    "ds_read_b64_tr_b16 v[108:109], v252 offset:40960\n"
    "ds_read_b64_tr_b16 v[110:111], v252 offset:41984\n"
    "v_xor_b32 v246, 0x1800, v255\n"
    "v_xor_b32 v247, 0x3800, v255\n"
    "v_exp_f32 v29, v29\n"
    "v_exp_f32 v30, v30\n"
    "v_exp_f32 v31, v31\n"
    "v_cvt_pk_bf16_f32 v102, v28, v29\n"
    "v_cvt_pk_bf16_f32 v103, v30, v31\n"
    "s_nop 1\n"
    "v_mfma_f32_32x32x16_bf16 v[208:223], v[60:63], v[100:103], v[208:223]\n"
    "s_branch .Lk_if27_end\n"
    ".Lk_if27:\n"
    "s_setprio 3\n"
    "s_waitcnt lgkmcnt(5)\n"
    "v_mfma_f32_32x32x16_bf16 v[16:31], v[32:35], v[120:123], 0\n"
    "v_exp_f32 v0, v0\n"
    "v_exp_f32 v1, v1\n"
    "v_exp_f32 v2, v2\n"
    "v_exp_f32 v3, v3\n"
    "v_cvt_pk_bf16_f32 v96, v0, v1\n"
    "v_exp_f32 v4, v4\n"
    "v_cvt_pk_bf16_f32 v97, v2, v3\n"
    "s_waitcnt lgkmcnt(4)\n"
    "v_mfma_f32_32x32x16_bf16 v[16:31], v[36:39], v[124:127], v[16:31]\n"
    "v_exp_f32 v5, v5\n"
    "v_exp_f32 v6, v6\n"
    "v_exp_f32 v7, v7\n"
    "v_cvt_pk_bf16_f32 v98, v4, v5\n"
    "s_setprio 2\n"
    "v_exp_f32 v8, v8\n"
    "v_cvt_pk_bf16_f32 v99, v6, v7\n"
    "v_exp_f32 v9, v9\n"
    "v_exp_f32 v10, v10\n"
    "s_waitcnt vmcnt(8)\n"
    "v_mfma_f32_32x32x16_bf16 v[160:175], v[48:51], v[96:99], 0\n"
    "v_exp_f32 v11, v11\n"
    "v_cvt_pk_bf16_f32 v100, v8, v9\n"
    "v_exp_f32 v12, v12\n"
    "v_cvt_pk_bf16_f32 v101, v10, v11\n"
    "v_exp_f32 v13, v13\n"
    "v_exp_f32 v14, v14\n"
    "v_exp_f32 v15, v15\n"
    "v_cvt_pk_bf16_f32 v102, v12, v13\n"
    "v_cvt_pk_bf16_f32 v103, v14, v15\n"
    "s_setprio 1\n"
    "s_waitcnt lgkmcnt(3)\n"
    "v_mfma_f32_32x32x16_bf16 v[0:15], v[32:35], v[224:227], 0\n"
    "v_exp_f32 v16, v16\n"
    "v_exp_f32 v17, v17\n"
    "v_exp_f32 v18, v18\n"
    "s_waitcnt vmcnt(7)\n"
    "v_mfma_f32_32x32x16_bf16 v[160:175], v[52:55], v[100:103], v[160:175]\n"
    "v_exp_f32 v19, v19\n"
    "v_cvt_pk_bf16_f32 v96, v16, v17\n"
    "v_exp_f32 v20, v20\n"
    "v_cvt_pk_bf16_f32 v97, v18, v19\n"
    "s_waitcnt lgkmcnt(2)\n"
    "v_mfma_f32_32x32x16_bf16 v[0:15], v[36:39], v[228:231], v[0:15]\n"
    "v_exp_f32 v21, v21\n"
    "v_exp_f32 v22, v22\n"
    "v_exp_f32 v23, v23\n"
    "v_cvt_pk_bf16_f32 v98, v20, v21\n"
    "s_setprio 0\n"
    "v_exp_f32 v24, v24\n"
    "v_cvt_pk_bf16_f32 v99, v22, v23\n"
    "v_exp_f32 v25, v25\n"
    "v_exp_f32 v26, v26\n"
    "v_mfma_f32_32x32x16_bf16 v[176:191], v[48:51], v[96:99], 0\n"
    "v_exp_f32 v27, v27\n"
    "v_cvt_pk_bf16_f32 v100, v24, v25\n"
    "v_exp_f32 v28, v28\n"
    "v_cvt_pk_bf16_f32 v101, v26, v27\n"
    "v_exp_f32 v29, v29\n"
    "v_exp_f32 v30, v30\n"
    "v_exp_f32 v31, v31\n"
    "v_cvt_pk_bf16_f32 v102, v28, v29\n"
    "v_cvt_pk_bf16_f32 v103, v30, v31\n"
    "s_setprio 3\n"
    "s_waitcnt lgkmcnt(1)\n"
    "v_mfma_f32_32x32x16_bf16 v[16:31], v[32:35], v[232:235], 0\n"
    "v_exp_f32 v0, v0\n"
    "v_exp_f32 v1, v1\n"
    "v_exp_f32 v2, v2\n"
    "v_mfma_f32_32x32x16_bf16 v[176:191], v[52:55], v[100:103], v[176:191]\n"
    "s_xor_b32 s47, s61, 0x400\n"
    "buffer_load_dwordx4 v[32:35], v245, s[20:23], s47 offen sc1\n"
    "v_exp_f32 v3, v3\n"
    "v_cvt_pk_bf16_f32 v96, v0, v1\n"
    "v_exp_f32 v4, v4\n"
    "v_cvt_pk_bf16_f32 v97, v2, v3\n"
    "s_waitcnt lgkmcnt(0)\n"
    "v_mfma_f32_32x32x16_bf16 v[16:31], v[36:39], v[236:239], v[16:31]\n"
    "s_xor_b32 s47, s62, 0x400\n"
    "buffer_load_dwordx4 v[36:39], v245, s[20:23], s47 offen sc1\n"
    "v_exp_f32 v5, v5\n"
    "v_exp_f32 v6, v6\n"
    "v_exp_f32 v7, v7\n"
    "v_cvt_pk_bf16_f32 v98, v4, v5\n"
    "s_setprio 2\n"
    "v_exp_f32 v8, v8\n"
    "v_cvt_pk_bf16_f32 v99, v6, v7\n"
    "v_exp_f32 v9, v9\n"
    "v_exp_f32 v10, v10\n"
    "v_mfma_f32_32x32x16_bf16 v[192:207], v[48:51], v[96:99], 0\n"
    "v_exp_f32 v11, v11\n"
    "v_cvt_pk_bf16_f32 v100, v8, v9\n"
    "v_exp_f32 v12, v12\n"
    "v_cvt_pk_bf16_f32 v101, v10, v11\n"
    "v_exp_f32 v13, v13\n"
    "v_exp_f32 v14, v14\n"
    "v_exp_f32 v15, v15\n"
    "v_cvt_pk_bf16_f32 v102, v12, v13\n"
    "v_cvt_pk_bf16_f32 v103, v14, v15\n"
    "s_setprio 1\n"
    "s_waitcnt vmcnt(8)\n"
    "v_mfma_f32_32x32x16_bf16 v[0:15], v[40:43], v[112:115], 0\n"
    "s_add_u32 s59, s59, 0x40000\n"
    "s_waitcnt vmcnt(2)\n"
    "s_mov_b64 exec, 1\n"
    "buffer_store_dwordx2 v[240:241], off, s[20:23], s59 sc1\n"
    "s_mov_b64 exec, -1\n"
    "v_exp_f32 v16, v16\n"
    "v_exp_f32 v17, v17\n"
    "v_exp_f32 v18, v18\n"
    "v_mfma_f32_32x32x16_bf16 v[192:207], v[52:55], v[100:103], v[192:207]\n"
    "v_exp_f32 v19, v19\n"
    "v_cvt_pk_bf16_f32 v96, v16, v17\n"
    "v_exp_f32 v20, v20\n"
    "v_cvt_pk_bf16_f32 v97, v18, v19\n"
    "v_mfma_f32_32x32x16_bf16 v[0:15], v[44:47], v[116:119], v[0:15]\n"
    "v_exp_f32 v21, v21\n"
    "v_exp_f32 v22, v22\n"
    "v_exp_f32 v23, v23\n"
    "v_cvt_pk_bf16_f32 v98, v20, v21\n"
    "s_setprio 0\n"
    "v_exp_f32 v24, v24\n"
    "v_cvt_pk_bf16_f32 v99, v22, v23\n"
    "v_exp_f32 v25, v25\n"
    "v_exp_f32 v26, v26\n"
    "v_mfma_f32_32x32x16_bf16 v[208:223], v[48:51], v[96:99], 0\n"
    "v_exp_f32 v27, v27\n"
    "v_cvt_pk_bf16_f32 v100, v24, v25\n"
    "v_exp_f32 v28, v28\n"
    "v_cvt_pk_bf16_f32 v101, v26, v27\n"
    "v_exp_f32 v29, v29\n"
    "v_exp_f32 v30, v30\n"
    "v_exp_f32 v31, v31\n"
    "v_cvt_pk_bf16_f32 v102, v28, v29\n"
    "v_cvt_pk_bf16_f32 v103, v30, v31\n"
    "s_setprio 3\n"
    "v_mfma_f32_32x32x16_bf16 v[16:31], v[40:43], v[120:123], 0\n"
    "v_exp_f32 v0, v0\n"
    "v_exp_f32 v1, v1\n"
    "v_exp_f32 v2, v2\n"
    "v_mfma_f32_32x32x16_bf16 v[208:223], v[52:55], v[100:103], v[208:223]\n"
    "s_xor_b32 s47, s63, 0x2000\n"
    "s_mov_b64 exec, s[36:37]\n"
    "buffer_load_dwordx4 v[48:51], v254, s[20:23], s47 offen sc1\n"
    "buffer_load_dwordx4 v[52:55], v254, s[20:23], s47 offen offset:2048 sc1\n"
    "s_mov_b64 exec, -1\n"
    "v_exp_f32 v3, v3\n"
    "v_cvt_pk_bf16_f32 v96, v0, v1\n"
    "v_exp_f32 v4, v4\n"
    "v_cvt_pk_bf16_f32 v97, v2, v3\n"
    "v_mfma_f32_32x32x16_bf16 v[16:31], v[44:47], v[124:127], v[16:31]\n"
    "v_exp_f32 v5, v5\n"
    "v_exp_f32 v6, v6\n"
    "v_exp_f32 v7, v7\n"
    "v_cvt_pk_bf16_f32 v98, v4, v5\n"
    "s_setprio 2\n"
    "v_exp_f32 v8, v8\n"
    "v_cvt_pk_bf16_f32 v99, v6, v7\n"
    "v_exp_f32 v9, v9\n"
    "v_exp_f32 v10, v10\n"
    "v_mfma_f32_32x32x16_bf16 v[160:175], v[56:59], v[96:99], v[160:175]\n"
    "v_exp_f32 v11, v11\n"
    "v_cvt_pk_bf16_f32 v100, v8, v9\n"
    "v_exp_f32 v12, v12\n"
    "v_cvt_pk_bf16_f32 v101, v10, v11\n"
    "v_exp_f32 v13, v13\n"
    "v_exp_f32 v14, v14\n"
    "v_exp_f32 v15, v15\n"
    "v_cvt_pk_bf16_f32 v102, v12, v13\n"
    "v_cvt_pk_bf16_f32 v103, v14, v15\n"
    "s_setprio 1\n"
    "v_mfma_f32_32x32x16_bf16 v[0:15], v[40:43], v[224:227], 0\n"
    "v_exp_f32 v16, v16\n"
    "v_exp_f32 v17, v17\n"
    "v_exp_f32 v18, v18\n"
    "v_mfma_f32_32x32x16_bf16 v[160:175], v[60:63], v[100:103], v[160:175]\n"
    "v_exp_f32 v19, v19\n"
    "v_cvt_pk_bf16_f32 v96, v16, v17\n"
    "v_exp_f32 v20, v20\n"
    "v_cvt_pk_bf16_f32 v97, v18, v19\n"
    "v_mfma_f32_32x32x16_bf16 v[0:15], v[44:47], v[228:231], v[0:15]\n"
    "v_exp_f32 v21, v21\n"
    "v_exp_f32 v22, v22\n"
    "v_exp_f32 v23, v23\n"
    "v_cvt_pk_bf16_f32 v98, v20, v21\n"
    "s_setprio 0\n"
    "v_exp_f32 v24, v24\n"
    "v_cvt_pk_bf16_f32 v99, v22, v23\n"
    "v_exp_f32 v25, v25\n"
    "v_exp_f32 v26, v26\n"
    "v_mfma_f32_32x32x16_bf16 v[176:191], v[56:59], v[96:99], v[176:191]\n"
    "v_exp_f32 v27, v27\n"
    "v_cvt_pk_bf16_f32 v100, v24, v25\n"
    "v_exp_f32 v28, v28\n"
    "v_cvt_pk_bf16_f32 v101, v26, v27\n"
    "v_exp_f32 v29, v29\n"
    "v_exp_f32 v30, v30\n"
    "v_exp_f32 v31, v31\n"
    "v_cvt_pk_bf16_f32 v102, v28, v29\n"
    "v_cvt_pk_bf16_f32 v103, v30, v31\n"
    "s_setprio 3\n"
    "v_mfma_f32_32x32x16_bf16 v[16:31], v[40:43], v[232:235], 0\n"
    "v_exp_f32 v0, v0\n"
    "v_exp_f32 v1, v1\n"
    "v_exp_f32 v2, v2\n"
    "v_mfma_f32_32x32x16_bf16 v[176:191], v[60:63], v[100:103], v[176:191]\n"
    "s_xor_b32 s47, s61, 0x600\n"
    "buffer_load_dwordx4 v[40:43], v245, s[20:23], s47 offen sc1\n"
    "v_exp_f32 v3, v3\n"
    "v_cvt_pk_bf16_f32 v96, v0, v1\n"
    "v_exp_f32 v4, v4\n"
    "v_cvt_pk_bf16_f32 v97, v2, v3\n"
    "v_mfma_f32_32x32x16_bf16 v[16:31], v[44:47], v[236:239], v[16:31]\n"
    "s_xor_b32 s47, s62, 0x600\n"
    "buffer_load_dwordx4 v[44:47], v245, s[20:23], s47 offen sc1\n"
    "v_exp_f32 v5, v5\n"
    "v_exp_f32 v6, v6\n"
    "v_exp_f32 v7, v7\n"
    "v_cvt_pk_bf16_f32 v98, v4, v5\n"
    "s_setprio 2\n"
    "v_exp_f32 v8, v8\n"
    "v_cvt_pk_bf16_f32 v99, v6, v7\n"
    "v_exp_f32 v9, v9\n"
    "v_exp_f32 v10, v10\n"
    "v_mfma_f32_32x32x16_bf16 v[192:207], v[56:59], v[96:99], v[192:207]\n"
    "v_exp_f32 v11, v11\n"
    "v_cvt_pk_bf16_f32 v100, v8, v9\n"
    "v_exp_f32 v12, v12\n"
    "v_cvt_pk_bf16_f32 v101, v10, v11\n"
    "v_exp_f32 v13, v13\n"
    "v_exp_f32 v14, v14\n"
    "v_exp_f32 v15, v15\n"
    "v_cvt_pk_bf16_f32 v102, v12, v13\n"
    "v_cvt_pk_bf16_f32 v103, v14, v15\n"
    "s_setprio 1\n"
    "s_waitcnt vmcnt(6)\n"
    "v_mfma_f32_32x32x16_bf16 v[0:15], v[32:35], v[112:115], 0\n"
    "v_exp_f32 v16, v16\n"
    "v_exp_f32 v17, v17\n"
    "v_exp_f32 v18, v18\n"
    "v_mfma_f32_32x32x16_bf16 v[192:207], v[60:63], v[100:103], v[192:207]\n"
    "v_exp_f32 v19, v19\n"
    "v_cvt_pk_bf16_f32 v96, v16, v17\n"
    "v_exp_f32 v20, v20\n"
    "v_cvt_pk_bf16_f32 v97, v18, v19\n"
    "s_waitcnt vmcnt(5)\n"
    "v_mfma_f32_32x32x16_bf16 v[0:15], v[36:39], v[116:119], v[0:15]\n"
    "v_exp_f32 v21, v21\n"
    "v_exp_f32 v22, v22\n"
    "v_exp_f32 v23, v23\n"
    "v_cvt_pk_bf16_f32 v98, v20, v21\n"
    "s_setprio 0\n"
    "v_exp_f32 v24, v24\n"
    "v_cvt_pk_bf16_f32 v99, v22, v23\n"
    "v_exp_f32 v25, v25\n"
    "v_exp_f32 v26, v26\n"
    "v_mfma_f32_32x32x16_bf16 v[208:223], v[56:59], v[96:99], v[208:223]\n"
    "v_exp_f32 v27, v27\n"
    "v_cvt_pk_bf16_f32 v100, v24, v25\n"
    "v_exp_f32 v28, v28\n"
    "v_cvt_pk_bf16_f32 v101, v26, v27\n"
    "v_exp_f32 v29, v29\n"
    "v_exp_f32 v30, v30\n"
    "v_exp_f32 v31, v31\n"
    "v_cvt_pk_bf16_f32 v102, v28, v29\n"
    "v_cvt_pk_bf16_f32 v103, v30, v31\n"
    "s_setprio 3\n"
    "v_mfma_f32_32x32x16_bf16 v[16:31], v[32:35], v[120:123], 0\n"
    "v_exp_f32 v0, v0\n"
    "v_exp_f32 v1, v1\n"
    "v_exp_f32 v2, v2\n"
    "v_mfma_f32_32x32x16_bf16 v[208:223], v[60:63], v[100:103], v[208:223]\n"
    "s_xor_b32 s47, s63, 0x3000\n"
    "s_mov_b64 exec, s[36:37]\n"
    "buffer_load_dwordx4 v[56:59], v254, s[20:23], s47 offen sc1\n"
    "buffer_load_dwordx4 v[60:63], v254, s[20:23], s47 offen offset:2048 sc1\n"
    "s_mov_b64 exec, -1\n"
    "v_exp_f32 v3, v3\n"
    "v_cvt_pk_bf16_f32 v96, v0, v1\n"
    "v_exp_f32 v4, v4\n"
    "v_cvt_pk_bf16_f32 v97, v2, v3\n"
    "v_mfma_f32_32x32x16_bf16 v[16:31], v[36:39], v[124:127], v[16:31]\n"
    "v_exp_f32 v5, v5\n"
    "v_exp_f32 v6, v6\n"
    "v_exp_f32 v7, v7\n"
    "v_cvt_pk_bf16_f32 v98, v4, v5\n"
    "s_setprio 2\n"
    "v_exp_f32 v8, v8\n"
    "v_cvt_pk_bf16_f32 v99, v6, v7\n"
    "v_exp_f32 v9, v9\n"
    "v_exp_f32 v10, v10\n"
    "s_waitcnt vmcnt(5)\n"
    "v_mfma_f32_32x32x16_bf16 v[160:175], v[48:51], v[96:99], v[160:175]\n"
    "v_exp_f32 v11, v11\n"
    "v_cvt_pk_bf16_f32 v100, v8, v9\n"
    "v_exp_f32 v12, v12\n"
    "v_cvt_pk_bf16_f32 v101, v10, v11\n"
    "v_exp_f32 v13, v13\n"
    "v_exp_f32 v14, v14\n"
    "v_exp_f32 v15, v15\n"
    "v_cvt_pk_bf16_f32 v102, v12, v13\n"
    "v_cvt_pk_bf16_f32 v103, v14, v15\n"
    "s_waitcnt lgkmcnt(0)\n"
    "s_barrier\n"
    "s_setprio 1\n"
    "v_mfma_f32_32x32x16_bf16 v[0:15], v[32:35], v[224:227], 0\n"
    "v_exp_f32 v16, v16\n"
    "v_exp_f32 v17, v17\n"
    "v_exp_f32 v18, v18\n"
    "s_waitcnt vmcnt(4)\n"
    "v_mfma_f32_32x32x16_bf16 v[160:175], v[52:55], v[100:103], v[160:175]\n"
    "v_exp_f32 v19, v19\n"
    "v_cvt_pk_bf16_f32 v96, v16, v17\n"
    "v_exp_f32 v20, v20\n"
    "v_cvt_pk_bf16_f32 v97, v18, v19\n"
    "v_mfma_f32_32x32x16_bf16 v[0:15], v[36:39], v[228:231], v[0:15]\n"
    "v_exp_f32 v21, v21\n"
    "v_exp_f32 v22, v22\n"
    "v_exp_f32 v23, v23\n"
    "v_cvt_pk_bf16_f32 v98, v20, v21\n"
    "s_setprio 0\n"
    "v_exp_f32 v24, v24\n"
    "v_cvt_pk_bf16_f32 v99, v22, v23\n"
    "v_exp_f32 v25, v25\n"
    "v_exp_f32 v26, v26\n"
    "v_mfma_f32_32x32x16_bf16 v[176:191], v[48:51], v[96:99], v[176:191]\n"
    "v_exp_f32 v27, v27\n"
    "v_cvt_pk_bf16_f32 v100, v24, v25\n"
    "v_exp_f32 v28, v28\n"
    "v_cvt_pk_bf16_f32 v101, v26, v27\n"
    "v_exp_f32 v29, v29\n"
    "v_exp_f32 v30, v30\n"
    "v_exp_f32 v31, v31\n"
    "v_cvt_pk_bf16_f32 v102, v28, v29\n"
    "v_cvt_pk_bf16_f32 v103, v30, v31\n"
    "s_setprio 3\n"
    "v_mfma_f32_32x32x16_bf16 v[16:31], v[32:35], v[232:235], 0\n"
    "v_exp_f32 v0, v0\n"
    "v_exp_f32 v1, v1\n"
    "v_exp_f32 v2, v2\n"
    "v_mfma_f32_32x32x16_bf16 v[176:191], v[52:55], v[100:103], v[176:191]\n"
    "s_xor_b32 s47, s61, 0x800\n"
    "buffer_load_dwordx4 v[32:35], v245, s[20:23], s47 offen sc1\n"
    "v_exp_f32 v3, v3\n"
    "v_cvt_pk_bf16_f32 v96, v0, v1\n"
    "v_exp_f32 v4, v4\n"
    "v_cvt_pk_bf16_f32 v97, v2, v3\n"
    "v_mfma_f32_32x32x16_bf16 v[16:31], v[36:39], v[236:239], v[16:31]\n"
    "s_xor_b32 s47, s62, 0x800\n"
    "buffer_load_dwordx4 v[36:39], v245, s[20:23], s47 offen sc1\n"
    "v_exp_f32 v5, v5\n"
    "v_exp_f32 v6, v6\n"
    "v_exp_f32 v7, v7\n"
    "v_cvt_pk_bf16_f32 v98, v4, v5\n"
    "s_setprio 2\n"
    "v_exp_f32 v8, v8\n"
    "v_cvt_pk_bf16_f32 v99, v6, v7\n"
    "v_exp_f32 v9, v9\n"
    "v_exp_f32 v10, v10\n"
    "v_mfma_f32_32x32x16_bf16 v[192:207], v[48:51], v[96:99], v[192:207]\n"
    "v_exp_f32 v11, v11\n"
    "v_cvt_pk_bf16_f32 v100, v8, v9\n"
    "v_exp_f32 v12, v12\n"
    "v_cvt_pk_bf16_f32 v101, v10, v11\n"
    "v_exp_f32 v13, v13\n"
    "v_exp_f32 v14, v14\n"
    "v_exp_f32 v15, v15\n"
    "v_cvt_pk_bf16_f32 v102, v12, v13\n"
    "v_cvt_pk_bf16_f32 v103, v14, v15\n"
    "s_setprio 1\n"
    "s_waitcnt vmcnt(5)\n"
    "v_mfma_f32_32x32x16_bf16 v[0:15], v[40:43], v[112:115], 0\n"
    "v_exp_f32 v16, v16\n"
    "v_exp_f32 v17, v17\n"
    "v_exp_f32 v18, v18\n"
    "v_mfma_f32_32x32x16_bf16 v[192:207], v[52:55], v[100:103], v[192:207]\n"
    "v_exp_f32 v19, v19\n"
    "v_cvt_pk_bf16_f32 v96, v16, v17\n"
    "v_exp_f32 v20, v20\n"
    "v_cvt_pk_bf16_f32 v97, v18, v19\n"
    "s_waitcnt vmcnt(4)\n"
    "v_mfma_f32_32x32x16_bf16 v[0:15], v[44:47], v[116:119], v[0:15]\n"
    "v_exp_f32 v21, v21\n"
    "v_exp_f32 v22, v22\n"
    "v_exp_f32 v23, v23\n"
    "v_cvt_pk_bf16_f32 v98, v20, v21\n"
    "s_setprio 0\n"
    "v_exp_f32 v24, v24\n"
    "v_cvt_pk_bf16_f32 v99, v22, v23\n"
    "v_exp_f32 v25, v25\n"
    "v_exp_f32 v26, v26\n"
    "v_mfma_f32_32x32x16_bf16 v[208:223], v[48:51], v[96:99], v[208:223]\n"
    "v_exp_f32 v27, v27\n"
    "v_cvt_pk_bf16_f32 v100, v24, v25\n"
    "v_exp_f32 v28, v28\n"
    "v_cvt_pk_bf16_f32 v101, v26, v27\n"
    "v_exp_f32 v29, v29\n"
    "v_exp_f32 v30, v30\n"
    "v_exp_f32 v31, v31\n"
    "v_cvt_pk_bf16_f32 v102, v28, v29\n"
    "v_cvt_pk_bf16_f32 v103, v30, v31\n"
    "s_setprio 3\n"
    "v_mfma_f32_32x32x16_bf16 v[16:31], v[40:43], v[120:123], 0\n"
    "v_exp_f32 v0, v0\n"
    "v_exp_f32 v1, v1\n"
    "v_exp_f32 v2, v2\n"
    "v_mfma_f32_32x32x16_bf16 v[208:223], v[52:55], v[100:103], v[208:223]\n"
    "s_xor_b32 s47, s63, 0x4000\n"
    "s_mov_b64 exec, s[36:37]\n"
    "buffer_load_dwordx4 v[48:51], v254, s[20:23], s47 offen sc1\n"
    "buffer_load_dwordx4 v[52:55], v254, s[20:23], s47 offen offset:2048 sc1\n"
    "s_mov_b64 exec, -1\n"
    "v_exp_f32 v3, v3\n"
    "v_cvt_pk_bf16_f32 v96, v0, v1\n"
    "v_exp_f32 v4, v4\n"
    "v_cvt_pk_bf16_f32 v97, v2, v3\n"
    "v_mfma_f32_32x32x16_bf16 v[16:31], v[44:47], v[124:127], v[16:31]\n"
    "v_exp_f32 v5, v5\n"
    "v_exp_f32 v6, v6\n"
    "v_exp_f32 v7, v7\n"
    "v_cvt_pk_bf16_f32 v98, v4, v5\n"
    "s_setprio 2\n"
    "v_exp_f32 v8, v8\n"
    "v_cvt_pk_bf16_f32 v99, v6, v7\n"
    "v_exp_f32 v9, v9\n"
    "v_exp_f32 v10, v10\n"
    "s_waitcnt vmcnt(5)\n"
    "v_mfma_f32_32x32x16_bf16 v[160:175], v[56:59], v[96:99], v[160:175]\n"
    "v_exp_f32 v11, v11\n"
    "v_cvt_pk_bf16_f32 v100, v8, v9\n"
    "v_exp_f32 v12, v12\n"
    "v_cvt_pk_bf16_f32 v101, v10, v11\n"
    "v_exp_f32 v13, v13\n"
    "v_exp_f32 v14, v14\n"
    "v_exp_f32 v15, v15\n"
    "v_cvt_pk_bf16_f32 v102, v12, v13\n"
    "v_cvt_pk_bf16_f32 v103, v14, v15\n"
    "s_setprio 1\n"
    "v_mfma_f32_32x32x16_bf16 v[0:15], v[40:43], v[224:227], 0\n"
    "v_exp_f32 v16, v16\n"
    "v_exp_f32 v17, v17\n"
    "v_exp_f32 v18, v18\n"
    "s_waitcnt vmcnt(4)\n"
    "v_mfma_f32_32x32x16_bf16 v[160:175], v[60:63], v[100:103], v[160:175]\n"
    "v_exp_f32 v19, v19\n"
    "v_cvt_pk_bf16_f32 v96, v16, v17\n"
    "v_exp_f32 v20, v20\n"
    "v_cvt_pk_bf16_f32 v97, v18, v19\n"
    "v_mfma_f32_32x32x16_bf16 v[0:15], v[44:47], v[228:231], v[0:15]\n"
    "v_exp_f32 v21, v21\n"
    "v_exp_f32 v22, v22\n"
    "v_exp_f32 v23, v23\n"
    "v_cvt_pk_bf16_f32 v98, v20, v21\n"
    "s_setprio 0\n"
    "v_exp_f32 v24, v24\n"
    "v_cvt_pk_bf16_f32 v99, v22, v23\n"
    "v_exp_f32 v25, v25\n"
    "v_exp_f32 v26, v26\n"
    "v_mfma_f32_32x32x16_bf16 v[176:191], v[56:59], v[96:99], v[176:191]\n"
    "v_exp_f32 v27, v27\n"
    "v_cvt_pk_bf16_f32 v100, v24, v25\n"
    "v_exp_f32 v28, v28\n"
    "v_cvt_pk_bf16_f32 v101, v26, v27\n"
    "v_exp_f32 v29, v29\n"
    "v_exp_f32 v30, v30\n"
    "v_exp_f32 v31, v31\n"
    "v_cvt_pk_bf16_f32 v102, v28, v29\n"
    "v_cvt_pk_bf16_f32 v103, v30, v31\n"
    "s_setprio 3\n"
    "v_mfma_f32_32x32x16_bf16 v[16:31], v[40:43], v[232:235], 0\n"
    "v_exp_f32 v0, v0\n"
    "v_exp_f32 v1, v1\n"
    "v_exp_f32 v2, v2\n"
    "v_mfma_f32_32x32x16_bf16 v[176:191], v[60:63], v[100:103], v[176:191]\n"
    "s_xor_b32 s47, s61, 0xa00\n"
    "buffer_load_dwordx4 v[40:43], v245, s[20:23], s47 offen sc1\n"
    "v_exp_f32 v3, v3\n"
    "v_cvt_pk_bf16_f32 v96, v0, v1\n"
    "v_exp_f32 v4, v4\n"
    "v_cvt_pk_bf16_f32 v97, v2, v3\n"
    "v_mfma_f32_32x32x16_bf16 v[16:31], v[44:47], v[236:239], v[16:31]\n"
    "s_xor_b32 s47, s62, 0xa00\n"
    "buffer_load_dwordx4 v[44:47], v245, s[20:23], s47 offen sc1\n"
    "v_exp_f32 v5, v5\n"
    "v_exp_f32 v6, v6\n"
    "v_exp_f32 v7, v7\n"
    "v_cvt_pk_bf16_f32 v98, v4, v5\n"
    "s_setprio 2\n"
    "v_exp_f32 v8, v8\n"
    "v_cvt_pk_bf16_f32 v99, v6, v7\n"
    "v_exp_f32 v9, v9\n"
    "v_exp_f32 v10, v10\n"
    "v_mfma_f32_32x32x16_bf16 v[192:207], v[56:59], v[96:99], v[192:207]\n"
    "v_exp_f32 v11, v11\n"
    "v_cvt_pk_bf16_f32 v100, v8, v9\n"
    "v_exp_f32 v12, v12\n"
    "v_cvt_pk_bf16_f32 v101, v10, v11\n"
    "v_exp_f32 v13, v13\n"
    "v_exp_f32 v14, v14\n"
    "v_exp_f32 v15, v15\n"
    "v_cvt_pk_bf16_f32 v102, v12, v13\n"
    "v_cvt_pk_bf16_f32 v103, v14, v15\n"
    "s_setprio 1\n"
    "s_waitcnt vmcnt(5)\n"
    "v_mfma_f32_32x32x16_bf16 v[0:15], v[32:35], v[112:115], 0\n"
    "v_exp_f32 v16, v16\n"
    "v_exp_f32 v17, v17\n"
    "v_exp_f32 v18, v18\n"
    "v_mfma_f32_32x32x16_bf16 v[192:207], v[60:63], v[100:103], v[192:207]\n"
    "v_exp_f32 v19, v19\n"
    "v_cvt_pk_bf16_f32 v96, v16, v17\n"
    "v_exp_f32 v20, v20\n"
    "v_cvt_pk_bf16_f32 v97, v18, v19\n"
    "s_waitcnt vmcnt(4)\n"
    "v_mfma_f32_32x32x16_bf16 v[0:15], v[36:39], v[116:119], v[0:15]\n"
    "v_exp_f32 v21, v21\n"
    "v_exp_f32 v22, v22\n"
    "v_exp_f32 v23, v23\n"
    "v_cvt_pk_bf16_f32 v98, v20, v21\n"
    "s_setprio 0\n"
    "v_exp_f32 v24, v24\n"
    "v_cvt_pk_bf16_f32 v99, v22, v23\n"
    "v_exp_f32 v25, v25\n"
    "v_exp_f32 v26, v26\n"
    "v_mfma_f32_32x32x16_bf16 v[208:223], v[56:59], v[96:99], v[208:223]\n"
    "v_exp_f32 v27, v27\n"
    "v_cvt_pk_bf16_f32 v100, v24, v25\n"
    "v_exp_f32 v28, v28\n"
    "v_cvt_pk_bf16_f32 v101, v26, v27\n"
    "v_exp_f32 v29, v29\n"
    "v_exp_f32 v30, v30\n"
    "v_exp_f32 v31, v31\n"
    "v_cvt_pk_bf16_f32 v102, v28, v29\n"
    "v_cvt_pk_bf16_f32 v103, v30, v31\n"
    "s_setprio 3\n"
    "v_mfma_f32_32x32x16_bf16 v[16:31], v[32:35], v[120:123], 0\n"
    "v_exp_f32 v0, v0\n"
    "v_exp_f32 v1, v1\n"
    "v_exp_f32 v2, v2\n"
    "v_mfma_f32_32x32x16_bf16 v[208:223], v[60:63], v[100:103], v[208:223]\n"
    "s_xor_b32 s47, s63, 0x5000\n"
    "s_mov_b64 exec, s[36:37]\n"
    "buffer_load_dwordx4 v[56:59], v254, s[20:23], s47 offen sc1\n"
    "buffer_load_dwordx4 v[60:63], v254, s[20:23], s47 offen offset:2048 sc1\n"
    "s_mov_b64 exec, -1\n"
    "v_exp_f32 v3, v3\n"
    "v_cvt_pk_bf16_f32 v96, v0, v1\n"
    "v_exp_f32 v4, v4\n"
    "v_cvt_pk_bf16_f32 v97, v2, v3\n"
    "v_mfma_f32_32x32x16_bf16 v[16:31], v[36:39], v[124:127], v[16:31]\n"
    "v_exp_f32 v5, v5\n"
    "v_exp_f32 v6, v6\n"
    "v_exp_f32 v7, v7\n"
    "v_cvt_pk_bf16_f32 v98, v4, v5\n"
    "s_setprio 2\n"
    "v_exp_f32 v8, v8\n"
    "v_cvt_pk_bf16_f32 v99, v6, v7\n"
    "v_exp_f32 v9, v9\n"
    "v_exp_f32 v10, v10\n"
    "s_waitcnt vmcnt(5)\n"
    "v_mfma_f32_32x32x16_bf16 v[160:175], v[48:51], v[96:99], v[160:175]\n"
    "v_exp_f32 v11, v11\n"
    "v_cvt_pk_bf16_f32 v100, v8, v9\n"
    "v_exp_f32 v12, v12\n"
    "v_cvt_pk_bf16_f32 v101, v10, v11\n"
    "v_exp_f32 v13, v13\n"
    "v_exp_f32 v14, v14\n"
    "v_exp_f32 v15, v15\n"
    "v_cvt_pk_bf16_f32 v102, v12, v13\n"
    "v_cvt_pk_bf16_f32 v103, v14, v15\n"
    "s_setprio 1\n"
    "v_mfma_f32_32x32x16_bf16 v[0:15], v[32:35], v[224:227], 0\n"
    "v_exp_f32 v16, v16\n"
    "v_exp_f32 v17, v17\n"
    "v_exp_f32 v18, v18\n"
    "s_waitcnt vmcnt(4)\n"
    "v_mfma_f32_32x32x16_bf16 v[160:175], v[52:55], v[100:103], v[160:175]\n"
    "v_exp_f32 v19, v19\n"
    "v_cvt_pk_bf16_f32 v96, v16, v17\n"
    "v_exp_f32 v20, v20\n"
    "v_cvt_pk_bf16_f32 v97, v18, v19\n"
    "v_mfma_f32_32x32x16_bf16 v[0:15], v[36:39], v[228:231], v[0:15]\n"
    "v_exp_f32 v21, v21\n"
    "v_exp_f32 v22, v22\n"
    "v_exp_f32 v23, v23\n"
    "v_cvt_pk_bf16_f32 v98, v20, v21\n"
    "s_setprio 0\n"
    "v_exp_f32 v24, v24\n"
    "v_cvt_pk_bf16_f32 v99, v22, v23\n"
    "v_exp_f32 v25, v25\n"
    "v_exp_f32 v26, v26\n"
    "v_mfma_f32_32x32x16_bf16 v[176:191], v[48:51], v[96:99], v[176:191]\n"
    "v_exp_f32 v27, v27\n"
    "v_cvt_pk_bf16_f32 v100, v24, v25\n"
    "v_exp_f32 v28, v28\n"
    "v_cvt_pk_bf16_f32 v101, v26, v27\n"
    "v_exp_f32 v29, v29\n"
    "v_exp_f32 v30, v30\n"
    "v_exp_f32 v31, v31\n"
    "v_cvt_pk_bf16_f32 v102, v28, v29\n"
    "v_cvt_pk_bf16_f32 v103, v30, v31\n"
    "s_setprio 3\n"
    "v_mfma_f32_32x32x16_bf16 v[16:31], v[32:35], v[232:235], 0\n"
    "v_exp_f32 v0, v0\n"
    "v_exp_f32 v1, v1\n"
    "v_exp_f32 v2, v2\n"
    "v_mfma_f32_32x32x16_bf16 v[176:191], v[52:55], v[100:103], v[176:191]\n"
    "s_xor_b32 s47, s61, 0xc00\n"
    "buffer_load_dwordx4 v[32:35], v245, s[20:23], s47 offen sc1\n"
    "v_exp_f32 v3, v3\n"
    "v_cvt_pk_bf16_f32 v96, v0, v1\n"
    "v_exp_f32 v4, v4\n"
    "v_cvt_pk_bf16_f32 v97, v2, v3\n"
    "v_mfma_f32_32x32x16_bf16 v[16:31], v[36:39], v[236:239], v[16:31]\n"
    "s_xor_b32 s47, s62, 0xc00\n"
    "buffer_load_dwordx4 v[36:39], v245, s[20:23], s47 offen sc1\n"
    "v_exp_f32 v5, v5\n"
    "v_exp_f32 v6, v6\n"
    "v_exp_f32 v7, v7\n"
    "v_cvt_pk_bf16_f32 v98, v4, v5\n"
    "s_setprio 2\n"
    "v_exp_f32 v8, v8\n"
    "v_cvt_pk_bf16_f32 v99, v6, v7\n"
    "v_exp_f32 v9, v9\n"
    "v_exp_f32 v10, v10\n"
    "v_mfma_f32_32x32x16_bf16 v[192:207], v[48:51], v[96:99], v[192:207]\n"
    "v_exp_f32 v11, v11\n"
    "v_cvt_pk_bf16_f32 v100, v8, v9\n"
    "v_exp_f32 v12, v12\n"
    "v_cvt_pk_bf16_f32 v101, v10, v11\n"
    "v_exp_f32 v13, v13\n"
    "v_exp_f32 v14, v14\n"
    "v_exp_f32 v15, v15\n"
    "v_cvt_pk_bf16_f32 v102, v12, v13\n"
    "v_cvt_pk_bf16_f32 v103, v14, v15\n"
    "s_setprio 1\n"
    "s_waitcnt vmcnt(5)\n"
    "v_mfma_f32_32x32x16_bf16 v[0:15], v[40:43], v[112:115], 0\n"
    "v_exp_f32 v16, v16\n"
    "v_exp_f32 v17, v17\n"
    "v_exp_f32 v18, v18\n"
    "v_mfma_f32_32x32x16_bf16 v[192:207], v[52:55], v[100:103], v[192:207]\n"
    "v_exp_f32 v19, v19\n"
    "v_cvt_pk_bf16_f32 v96, v16, v17\n"
    "v_exp_f32 v20, v20\n"
    "v_cvt_pk_bf16_f32 v97, v18, v19\n"
    "s_waitcnt vmcnt(4)\n"
    "v_mfma_f32_32x32x16_bf16 v[0:15], v[44:47], v[116:119], v[0:15]\n"
    "v_exp_f32 v21, v21\n"
    "v_exp_f32 v22, v22\n"
    "v_exp_f32 v23, v23\n"
    "v_cvt_pk_bf16_f32 v98, v20, v21\n"
    "s_setprio 0\n"
    "v_exp_f32 v24, v24\n"
    "v_cvt_pk_bf16_f32 v99, v22, v23\n"
    "v_exp_f32 v25, v25\n"
    "v_exp_f32 v26, v26\n"
    "v_mfma_f32_32x32x16_bf16 v[208:223], v[48:51], v[96:99], v[208:223]\n"
    "v_exp_f32 v27, v27\n"
    "v_cvt_pk_bf16_f32 v100, v24, v25\n"
    "v_exp_f32 v28, v28\n"
    "v_cvt_pk_bf16_f32 v101, v26, v27\n"
    "v_exp_f32 v29, v29\n"
    "v_exp_f32 v30, v30\n"
    "v_exp_f32 v31, v31\n"
    "v_cvt_pk_bf16_f32 v102, v28, v29\n"
    "v_cvt_pk_bf16_f32 v103, v30, v31\n"
    "s_setprio 3\n"
    "v_mfma_f32_32x32x16_bf16 v[16:31], v[40:43], v[120:123], 0\n"
    "v_exp_f32 v0, v0\n"
    "v_exp_f32 v1, v1\n"
    "v_exp_f32 v2, v2\n"
    "v_mfma_f32_32x32x16_bf16 v[208:223], v[52:55], v[100:103], v[208:223]\n"
    "s_xor_b32 s47, s63, 0x6000\n"
    "s_mov_b64 exec, s[36:37]\n"
    "buffer_load_dwordx4 v[48:51], v254, s[20:23], s47 offen sc1\n"
    "buffer_load_dwordx4 v[52:55], v254, s[20:23], s47 offen offset:2048 sc1\n"
    "s_mov_b64 exec, -1\n"
    "v_exp_f32 v3, v3\n"
    "v_cvt_pk_bf16_f32 v96, v0, v1\n"
    "v_exp_f32 v4, v4\n"
    "v_cvt_pk_bf16_f32 v97, v2, v3\n"
    "v_mfma_f32_32x32x16_bf16 v[16:31], v[44:47], v[124:127], v[16:31]\n"
    "v_exp_f32 v5, v5\n"
    "v_exp_f32 v6, v6\n"
    "v_exp_f32 v7, v7\n"
    "v_cvt_pk_bf16_f32 v98, v4, v5\n"
    "s_setprio 2\n"
    "v_exp_f32 v8, v8\n"
    "v_cvt_pk_bf16_f32 v99, v6, v7\n"
    "v_exp_f32 v9, v9\n"
    "v_exp_f32 v10, v10\n"
    "s_waitcnt vmcnt(5)\n"
    "v_mfma_f32_32x32x16_bf16 v[160:175], v[56:59], v[96:99], v[160:175]\n"
    "v_exp_f32 v11, v11\n"
    "v_cvt_pk_bf16_f32 v100, v8, v9\n"
    "v_exp_f32 v12, v12\n"
    "v_cvt_pk_bf16_f32 v101, v10, v11\n"
    "v_exp_f32 v13, v13\n"
    "v_exp_f32 v14, v14\n"
    "v_exp_f32 v15, v15\n"
    "v_cvt_pk_bf16_f32 v102, v12, v13\n"
    "v_cvt_pk_bf16_f32 v103, v14, v15\n"
    "s_setprio 1\n"
    "v_mfma_f32_32x32x16_bf16 v[0:15], v[40:43], v[224:227], 0\n"
    "v_exp_f32 v16, v16\n"
    "v_exp_f32 v17, v17\n"
    "v_exp_f32 v18, v18\n"
    "s_waitcnt vmcnt(4)\n"
    "v_mfma_f32_32x32x16_bf16 v[160:175], v[60:63], v[100:103], v[160:175]\n"
    "v_exp_f32 v19, v19\n"
    "v_cvt_pk_bf16_f32 v96, v16, v17\n"
    "v_exp_f32 v20, v20\n"
    "v_cvt_pk_bf16_f32 v97, v18, v19\n"
    "v_mfma_f32_32x32x16_bf16 v[0:15], v[44:47], v[228:231], v[0:15]\n"
    "v_exp_f32 v21, v21\n"
    "v_exp_f32 v22, v22\n"
    "v_exp_f32 v23, v23\n"
    "v_cvt_pk_bf16_f32 v98, v20, v21\n"
    "s_setprio 0\n"
    "v_exp_f32 v24, v24\n"
    "v_cvt_pk_bf16_f32 v99, v22, v23\n"
    "v_exp_f32 v25, v25\n"
    "v_exp_f32 v26, v26\n"
    "v_mfma_f32_32x32x16_bf16 v[176:191], v[56:59], v[96:99], v[176:191]\n"
    "v_exp_f32 v27, v27\n"
    "v_cvt_pk_bf16_f32 v100, v24, v25\n"
    "v_exp_f32 v28, v28\n"
    "v_cvt_pk_bf16_f32 v101, v26, v27\n"
    "v_exp_f32 v29, v29\n"
    "v_exp_f32 v30, v30\n"
    "v_exp_f32 v31, v31\n"
    "v_cvt_pk_bf16_f32 v102, v28, v29\n"
    "v_cvt_pk_bf16_f32 v103, v30, v31\n"
    "s_setprio 3\n"
    "v_mfma_f32_32x32x16_bf16 v[16:31], v[40:43], v[232:235], 0\n"
    "v_exp_f32 v0, v0\n"
    "v_exp_f32 v1, v1\n"
    "v_exp_f32 v2, v2\n"
    "v_mfma_f32_32x32x16_bf16 v[176:191], v[60:63], v[100:103], v[176:191]\n"
    "s_xor_b32 s47, s61, 0xe00\n"
    "buffer_load_dwordx4 v[40:43], v245, s[20:23], s47 offen sc1\n"
    "v_exp_f32 v3, v3\n"
    "v_cvt_pk_bf16_f32 v96, v0, v1\n"
    "v_exp_f32 v4, v4\n"
    "v_cvt_pk_bf16_f32 v97, v2, v3\n"
    "v_mfma_f32_32x32x16_bf16 v[16:31], v[44:47], v[236:239], v[16:31]\n"
    "s_xor_b32 s47, s62, 0xe00\n"
    "buffer_load_dwordx4 v[44:47], v245, s[20:23], s47 offen sc1\n"
    "v_exp_f32 v5, v5\n"
    "v_exp_f32 v6, v6\n"
    "v_exp_f32 v7, v7\n"
    "v_cvt_pk_bf16_f32 v98, v4, v5\n"
    "s_setprio 2\n"
    "v_exp_f32 v8, v8\n"
    "v_cvt_pk_bf16_f32 v99, v6, v7\n"
    "v_exp_f32 v9, v9\n"
    "v_exp_f32 v10, v10\n"
    "v_mfma_f32_32x32x16_bf16 v[192:207], v[56:59], v[96:99], v[192:207]\n"
    "v_exp_f32 v11, v11\n"
    "v_cvt_pk_bf16_f32 v100, v8, v9\n"
    "v_exp_f32 v12, v12\n"
    "v_cvt_pk_bf16_f32 v101, v10, v11\n"
    "v_exp_f32 v13, v13\n"
    "v_exp_f32 v14, v14\n"
    "v_exp_f32 v15, v15\n"
    "v_cvt_pk_bf16_f32 v102, v12, v13\n"
    "v_cvt_pk_bf16_f32 v103, v14, v15\n"
    "s_setprio 1\n"
    "s_waitcnt vmcnt(5)\n"
    "v_mfma_f32_32x32x16_bf16 v[0:15], v[32:35], v[112:115], 0\n"
    "v_exp_f32 v16, v16\n"
    "v_exp_f32 v17, v17\n"
    "v_exp_f32 v18, v18\n"
    "v_mfma_f32_32x32x16_bf16 v[192:207], v[60:63], v[100:103], v[192:207]\n"
    "v_exp_f32 v19, v19\n"
    "v_cvt_pk_bf16_f32 v96, v16, v17\n"
    "v_exp_f32 v20, v20\n"
    "v_cvt_pk_bf16_f32 v97, v18, v19\n"
    "s_waitcnt vmcnt(4)\n"
    "v_mfma_f32_32x32x16_bf16 v[0:15], v[36:39], v[116:119], v[0:15]\n"
    "v_exp_f32 v21, v21\n"
    "v_exp_f32 v22, v22\n"
    "v_exp_f32 v23, v23\n"
    "v_cvt_pk_bf16_f32 v98, v20, v21\n"
    "s_setprio 0\n"
    "v_exp_f32 v24, v24\n"
    "v_cvt_pk_bf16_f32 v99, v22, v23\n"
    "v_exp_f32 v25, v25\n"
    "v_exp_f32 v26, v26\n"
    "v_mfma_f32_32x32x16_bf16 v[208:223], v[56:59], v[96:99], v[208:223]\n"
    "v_exp_f32 v27, v27\n"
    "v_cvt_pk_bf16_f32 v100, v24, v25\n"
    "v_exp_f32 v28, v28\n"
    "v_cvt_pk_bf16_f32 v101, v26, v27\n"
    "v_exp_f32 v29, v29\n"
    "v_exp_f32 v30, v30\n"
    "v_exp_f32 v31, v31\n"
    "v_cvt_pk_bf16_f32 v102, v28, v29\n"
    "v_cvt_pk_bf16_f32 v103, v30, v31\n"
    "s_setprio 3\n"
    "v_mfma_f32_32x32x16_bf16 v[16:31], v[32:35], v[120:123], 0\n"
    "v_exp_f32 v0, v0\n"
    "v_exp_f32 v1, v1\n"
    "v_exp_f32 v2, v2\n"
    "v_mfma_f32_32x32x16_bf16 v[208:223], v[60:63], v[100:103], v[208:223]\n"
    "s_xor_b32 s47, s63, 0x7000\n"
    "s_mov_b64 exec, s[36:37]\n"
    "buffer_load_dwordx4 v[56:59], v254, s[20:23], s47 offen sc1\n"
    "buffer_load_dwordx4 v[60:63], v254, s[20:23], s47 offen offset:2048 sc1\n"
    "s_mov_b64 exec, -1\n"
    "v_exp_f32 v3, v3\n"
    "v_cvt_pk_bf16_f32 v96, v0, v1\n"
    "v_exp_f32 v4, v4\n"
    "v_cvt_pk_bf16_f32 v97, v2, v3\n"
    "v_mfma_f32_32x32x16_bf16 v[16:31], v[36:39], v[124:127], v[16:31]\n"
    "v_exp_f32 v5, v5\n"
    "v_exp_f32 v6, v6\n"
    "v_exp_f32 v7, v7\n"
    "v_cvt_pk_bf16_f32 v98, v4, v5\n"
    "s_setprio 2\n"
    "v_exp_f32 v8, v8\n"
    "v_cvt_pk_bf16_f32 v99, v6, v7\n"
    "v_exp_f32 v9, v9\n"
    "v_exp_f32 v10, v10\n"
    "s_waitcnt vmcnt(5)\n"
    "v_mfma_f32_32x32x16_bf16 v[160:175], v[48:51], v[96:99], v[160:175]\n"
    "v_exp_f32 v11, v11\n"
    "v_cvt_pk_bf16_f32 v100, v8, v9\n"
    "v_exp_f32 v12, v12\n"
    "v_cvt_pk_bf16_f32 v101, v10, v11\n"
    "v_exp_f32 v13, v13\n"
    "v_exp_f32 v14, v14\n"
    "v_exp_f32 v15, v15\n"
    "v_cvt_pk_bf16_f32 v102, v12, v13\n"
    "v_cvt_pk_bf16_f32 v103, v14, v15\n"
    "s_setprio 1\n"
    "s_waitcnt vmcnt(3)\n"
    "v_mfma_f32_32x32x16_bf16 v[0:15], v[40:43], v[112:115], 0\n"
    "v_exp_f32 v16, v16\n"
    "v_exp_f32 v17, v17\n"
    "v_exp_f32 v18, v18\n"
    "v_mfma_f32_32x32x16_bf16 v[160:175], v[52:55], v[100:103], v[160:175]\n"
    "v_exp_f32 v19, v19\n"
    "v_cvt_pk_bf16_f32 v96, v16, v17\n"
    "v_exp_f32 v20, v20\n"
    "v_cvt_pk_bf16_f32 v97, v18, v19\n"
    "s_waitcnt vmcnt(2)\n"
    "v_mfma_f32_32x32x16_bf16 v[0:15], v[44:47], v[116:119], v[0:15]\n"
    "v_exp_f32 v21, v21\n"
    "v_exp_f32 v22, v22\n"
    "v_exp_f32 v23, v23\n"
    "v_cvt_pk_bf16_f32 v98, v20, v21\n"
    "s_setprio 0\n"
    "v_exp_f32 v24, v24\n"
    "v_cvt_pk_bf16_f32 v99, v22, v23\n"
    "v_exp_f32 v25, v25\n"
    "v_exp_f32 v26, v26\n"
    "v_mfma_f32_32x32x16_bf16 v[176:191], v[48:51], v[96:99], v[176:191]\n"
    "v_exp_f32 v27, v27\n"
    "v_cvt_pk_bf16_f32 v100, v24, v25\n"
    "v_exp_f32 v28, v28\n"
    "v_cvt_pk_bf16_f32 v101, v26, v27\n"
    "v_exp_f32 v29, v29\n"
    "v_exp_f32 v30, v30\n"
    "v_exp_f32 v31, v31\n"
    "v_cvt_pk_bf16_f32 v102, v28, v29\n"
    "v_cvt_pk_bf16_f32 v103, v30, v31\n"
    "s_setprio 3\n"
    "v_mfma_f32_32x32x16_bf16 v[16:31], v[40:43], v[120:123], 0\n"
    "v_exp_f32 v0, v0\n"
    "v_exp_f32 v1, v1\n"
    "v_exp_f32 v2, v2\n"
    "v_mfma_f32_32x32x16_bf16 v[176:191], v[52:55], v[100:103], v[176:191]\n"
    "v_exp_f32 v3, v3\n"
    "v_cvt_pk_bf16_f32 v96, v0, v1\n"
    "v_exp_f32 v4, v4\n"
    "v_cvt_pk_bf16_f32 v97, v2, v3\n"
    "v_mfma_f32_32x32x16_bf16 v[16:31], v[44:47], v[124:127], v[16:31]\n"
    "v_exp_f32 v5, v5\n"
    "v_exp_f32 v6, v6\n"
    "v_exp_f32 v7, v7\n"
    "v_cvt_pk_bf16_f32 v98, v4, v5\n"
    "s_setprio 2\n"
    "v_exp_f32 v8, v8\n"
    "v_cvt_pk_bf16_f32 v99, v6, v7\n"
    "v_exp_f32 v9, v9\n"
    "v_exp_f32 v10, v10\n"
    "s_waitcnt vmcnt(1)\n"
    "v_mfma_f32_32x32x16_bf16 v[160:175], v[56:59], v[96:99], v[160:175]\n"
    "v_exp_f32 v11, v11\n"
    "v_cvt_pk_bf16_f32 v100, v8, v9\n"
    "v_exp_f32 v12, v12\n"
    "v_cvt_pk_bf16_f32 v101, v10, v11\n"
    "v_exp_f32 v13, v13\n"
    "v_exp_f32 v14, v14\n"
    "v_exp_f32 v15, v15\n"
    "v_cvt_pk_bf16_f32 v102, v12, v13\n"
    "v_cvt_pk_bf16_f32 v103, v14, v15\n"
    "s_setprio 1\n"
    "v_mfma_f32_32x32x16_bf16 v[0:15], v[32:35], v[224:227], 0\n"
    "v_exp_f32 v16, v16\n"
    "v_exp_f32 v17, v17\n"
    "v_exp_f32 v18, v18\n"
    "s_waitcnt vmcnt(0)\n"
    "v_mfma_f32_32x32x16_bf16 v[160:175], v[60:63], v[100:103], v[160:175]\n"
    "v_exp_f32 v19, v19\n"
    "v_cvt_pk_bf16_f32 v96, v16, v17\n"
    "v_exp_f32 v20, v20\n"
    "v_cvt_pk_bf16_f32 v97, v18, v19\n"
    "v_mfma_f32_32x32x16_bf16 v[0:15], v[36:39], v[228:231], v[0:15]\n"
    "v_exp_f32 v21, v21\n"
    "v_exp_f32 v22, v22\n"
    "v_exp_f32 v23, v23\n"
    "v_cvt_pk_bf16_f32 v98, v20, v21\n"
    "s_setprio 0\n"
    "v_exp_f32 v24, v24\n"
    "v_cvt_pk_bf16_f32 v99, v22, v23\n"
    "v_exp_f32 v25, v25\n"
    "v_exp_f32 v26, v26\n"
    "v_mfma_f32_32x32x16_bf16 v[176:191], v[56:59], v[96:99], v[176:191]\n"
    "v_exp_f32 v27, v27\n"
    "v_cvt_pk_bf16_f32 v100, v24, v25\n"
    "v_exp_f32 v28, v28\n"
    "v_cvt_pk_bf16_f32 v101, v26, v27\n"
    "v_xor_b32 v246, 0x800, v255\n"
    "ds_write_b128 v246, v[160:163]\n"
    "v_exp_f32 v29, v29\n"
    "v_exp_f32 v30, v30\n"
    "v_exp_f32 v31, v31\n"
    "v_cvt_pk_bf16_f32 v102, v28, v29\n"
    "v_cvt_pk_bf16_f32 v103, v30, v31\n"
    "s_setprio 3\n"
    "v_mfma_f32_32x32x16_bf16 v[16:31], v[40:43], v[224:227], 0\n"
    "ds_write_b128 v246, v[164:167] offset:1024\n"
    "v_exp_f32 v0, v0\n"
    "v_exp_f32 v1, v1\n"
    "v_exp_f32 v2, v2\n"
    "v_mfma_f32_32x32x16_bf16 v[176:191], v[60:63], v[100:103], v[176:191]\n"
    "v_xor_b32 v247, 0x2800, v255\n"
    "ds_write_b128 v247, v[168:171] offset:16384\n"
    "v_exp_f32 v3, v3\n"
    "v_cvt_pk_bf16_f32 v96, v0, v1\n"
    "v_exp_f32 v4, v4\n"
    "v_cvt_pk_bf16_f32 v97, v2, v3\n"
    "v_mfma_f32_32x32x16_bf16 v[16:31], v[44:47], v[228:231], v[16:31]\n"
    "v_exp_f32 v5, v5\n"
    "v_exp_f32 v6, v6\n"
    "v_exp_f32 v7, v7\n"
    "v_cvt_pk_bf16_f32 v98, v4, v5\n"
    "s_setprio 2\n"
    "ds_write_b128 v247, v[172:175] offset:17408\n"
    "v_exp_f32 v8, v8\n"
    "v_cvt_pk_bf16_f32 v99, v6, v7\n"
    "v_exp_f32 v9, v9\n"
    "v_exp_f32 v10, v10\n"
    "v_mfma_f32_32x32x16_bf16 v[192:207], v[48:51], v[96:99], v[192:207]\n"
    "v_exp_f32 v11, v11\n"
    "v_cvt_pk_bf16_f32 v100, v8, v9\n"
    "v_exp_f32 v12, v12\n"
    "v_cvt_pk_bf16_f32 v101, v10, v11\n"
    "v_xor_b32 v246, 0x1000, v255\n"
    "ds_write_b128 v246, v[176:179] offset:32768\n"
    "v_exp_f32 v13, v13\n"
    "v_exp_f32 v14, v14\n"
    "v_exp_f32 v15, v15\n"
    "v_cvt_pk_bf16_f32 v102, v12, v13\n"
    "v_cvt_pk_bf16_f32 v103, v14, v15\n"
    "s_setprio 1\n"
    "v_mfma_f32_32x32x16_bf16 v[0:15], v[32:35], v[232:235], 0\n"
    "ds_write_b128 v246, v[180:183] offset:33792\n"
    "v_exp_f32 v16, v16\n"
    "v_exp_f32 v17, v17\n"
    "v_exp_f32 v18, v18\n"
    "v_mfma_f32_32x32x16_bf16 v[192:207], v[52:55], v[100:103], v[192:207]\n"
    "v_xor_b32 v247, 0x3000, v255\n"
    "ds_write_b128 v247, v[184:187] offset:49152\n"
    "v_exp_f32 v19, v19\n"
    "v_cvt_pk_bf16_f32 v96, v16, v17\n"
    "v_exp_f32 v20, v20\n"
    "v_cvt_pk_bf16_f32 v97, v18, v19\n"
    "v_mfma_f32_32x32x16_bf16 v[0:15], v[36:39], v[236:239], v[0:15]\n"
    "v_exp_f32 v21, v21\n"
    "v_exp_f32 v22, v22\n"
    "v_exp_f32 v23, v23\n"
    "v_cvt_pk_bf16_f32 v98, v20, v21\n"
    "s_setprio 0\n"
    "ds_write_b128 v247, v[188:191] offset:50176\n"
    "v_exp_f32 v24, v24\n"
    "v_cvt_pk_bf16_f32 v99, v22, v23\n"
    "v_exp_f32 v25, v25\n"
    "v_exp_f32 v26, v26\n"
    "v_mfma_f32_32x32x16_bf16 v[192:207], v[56:59], v[96:99], v[192:207]\n"
    "v_exp_f32 v27, v27\n"
    "v_cvt_pk_bf16_f32 v100, v24, v25\n"
    "v_exp_f32 v28, v28\n"
    "v_cvt_pk_bf16_f32 v101, v26, v27\n"
    "v_exp_f32 v29, v29\n"
    "v_exp_f32 v30, v30\n"
    "v_exp_f32 v31, v31\n"
    "v_cvt_pk_bf16_f32 v102, v28, v29\n"
    "v_cvt_pk_bf16_f32 v103, v30, v31\n"
    "s_waitcnt lgkmcnt(0)\n"
    "s_barrier\n"
    "s_setprio 3\n"
    "v_mfma_f32_32x32x16_bf16 v[16:31], v[40:43], v[232:235], 0\n"
    "ds_read_b128 v[160:163], v255\n"
    "ds_read_b128 v[164:167], v255 offset:1024\n"
    "ds_read_b128 v[168:171], v255 offset:16384\n"
    "ds_read_b128 v[172:175], v255 offset:17408\n"
    "v_exp_f32 v0, v0\n"
    "v_exp_f32 v1, v1\n"
    "v_exp_f32 v2, v2\n"
    "v_mfma_f32_32x32x16_bf16 v[192:207], v[60:63], v[100:103], v[192:207]\n"
    "ds_read_b128 v[176:179], v255 offset:32768\n"
    "ds_read_b128 v[180:183], v255 offset:33792\n"
    "ds_read_b128 v[184:187], v255 offset:49152\n"
    "ds_read_b128 v[188:191], v255 offset:50176\n"
    "v_exp_f32 v3, v3\n"
    "v_cvt_pk_bf16_f32 v96, v0, v1\n"
    "v_exp_f32 v4, v4\n"
    "v_cvt_pk_bf16_f32 v97, v2, v3\n"
    "v_mfma_f32_32x32x16_bf16 v[16:31], v[44:47], v[236:239], v[16:31]\n"
    "v_exp_f32 v5, v5\n"
    "v_exp_f32 v6, v6\n"
    "v_exp_f32 v7, v7\n"
    "v_cvt_pk_bf16_f32 v98, v4, v5\n"
    "s_setprio 2\n"
    "v_exp_f32 v8, v8\n"
    "v_cvt_pk_bf16_f32 v99, v6, v7\n"
    "v_exp_f32 v9, v9\n"
    "v_exp_f32 v10, v10\n"
    "s_waitcnt lgkmcnt(5)\n"
    "v_pk_add_f32 v[160:161], v[160:161], v[168:169]\n"
    "v_pk_add_f32 v[162:163], v[162:163], v[170:171]\n"
    "s_waitcnt lgkmcnt(4)\n"
    "v_pk_add_f32 v[164:165], v[164:165], v[172:173]\n"
    "v_pk_add_f32 v[166:167], v[166:167], v[174:175]\n"
    "v_mfma_f32_32x32x16_bf16 v[208:223], v[48:51], v[96:99], v[208:223]\n"
    "v_exp_f32 v11, v11\n"
    "v_cvt_pk_bf16_f32 v100, v8, v9\n"
    "v_exp_f32 v12, v12\n"
    "v_cvt_pk_bf16_f32 v101, v10, v11\n"
    "ds_write_b128 v255, v[192:195]\n"
    "v_exp_f32 v13, v13\n"
    "v_exp_f32 v14, v14\n"
    "v_exp_f32 v15, v15\n"
    "v_cvt_pk_bf16_f32 v102, v12, v13\n"
    "v_cvt_pk_bf16_f32 v103, v14, v15\n"
    "s_waitcnt lgkmcnt(2)\n"
    "v_pk_add_f32 v[176:177], v[176:177], v[184:185]\n"
    "v_pk_add_f32 v[178:179], v[178:179], v[186:187]\n"
    "s_waitcnt lgkmcnt(1)\n"
    "v_pk_add_f32 v[180:181], v[180:181], v[188:189]\n"
    "v_pk_add_f32 v[182:183], v[182:183], v[190:191]\n"
    "s_setprio 1\n"
    "ds_write_b128 v255, v[196:199] offset:1024\n"
    "v_exp_f32 v16, v16\n"
    "v_exp_f32 v17, v17\n"
    "v_exp_f32 v18, v18\n"
    "v_mfma_f32_32x32x16_bf16 v[208:223], v[52:55], v[100:103], v[208:223]\n"
    "v_exp_f32 v19, v19\n"
    "v_cvt_pk_bf16_f32 v96, v16, v17\n"
    "v_exp_f32 v20, v20\n"
    "v_cvt_pk_bf16_f32 v97, v18, v19\n"
    "v_pk_add_f32 v[160:161], v[160:161], v[176:177]\n"
    "v_pk_add_f32 v[162:163], v[162:163], v[178:179]\n"
    "v_pk_add_f32 v[164:165], v[164:165], v[180:181]\n"
    "v_pk_add_f32 v[166:167], v[166:167], v[182:183]\n"
    "ds_write_b128 v255, v[200:203] offset:16384\n"
    "v_exp_f32 v21, v21\n"
    "v_exp_f32 v22, v22\n"
    "v_exp_f32 v23, v23\n"
    "v_cvt_pk_bf16_f32 v98, v20, v21\n"
    "s_setprio 0\n"
    "v_exp_f32 v24, v24\n"
    "v_cvt_pk_bf16_f32 v99, v22, v23\n"
    "v_exp_f32 v25, v25\n"
    "v_exp_f32 v26, v26\n"
    "v_mfma_f32_32x32x16_bf16 v[208:223], v[56:59], v[96:99], v[208:223]\n"
    "ds_write_b128 v255, v[204:207] offset:17408\n"
    "v_exp_f32 v27, v27\n"
    "v_cvt_pk_bf16_f32 v100, v24, v25\n"
    "v_exp_f32 v28, v28\n"
    "v_cvt_pk_bf16_f32 v101, v26, v27\n"
    "v_xor_b32 v246, 0x1800, v255\n"
    "v_xor_b32 v247, 0x3800, v255\n"
    "v_exp_f32 v29, v29\n"
    "v_exp_f32 v30, v30\n"
    "v_exp_f32 v31, v31\n"
    "v_cvt_pk_bf16_f32 v102, v28, v29\n"
    "v_cvt_pk_bf16_f32 v103, v30, v31\n"
    "s_nop 1\n"
    "v_mfma_f32_32x32x16_bf16 v[208:223], v[60:63], v[100:103], v[208:223]\n"
    ".Lk_if27_end:\n"
    "s_setprio 0\n"
    "v_xor_b32 v248, 0x2000, v255\n"
    "v_mov_b32 v24, 0x21800000\n"
    "v_mov_b32 v25, 0x5d800000\n"
    "s_nop 7\n"
    "ds_write_b128 v255, v[220:223] offset:33792\n"
    "ds_write_b128 v255, v[216:219] offset:32768\n"
    "v_pk_add_f32 v[208:209], v[208:209], v[160:161]\n"
    "v_pk_add_f32 v[210:211], v[210:211], v[162:163]\n"
    "v_pk_add_f32 v[212:213], v[212:213], v[164:165]\n"
    "v_pk_add_f32 v[214:215], v[214:215], v[166:167]\n"
    "s_waitcnt vmcnt(1) lgkmcnt(0)\n"
    "s_barrier\n"
    "ds_read_b128 v[196:199], v246 offset:1024\n"
    "ds_read_b128 v[204:207], v247 offset:17408\n"
    "ds_read_b128 v[20:23], v248 offset:33792\n"
    "ds_read_b128 v[192:195], v246\n"
    "ds_read_b128 v[200:203], v247 offset:16384\n"
    "ds_read_b128 v[16:19], v248 offset:32768\n"
    "s_waitcnt lgkmcnt(5)\n"
    "v_pk_add_f32 v[214:215], v[214:215], v[198:199]\n"
    "v_pk_add_f32 v[212:213], v[212:213], v[196:197]\n"
    "s_waitcnt lgkmcnt(4)\n"
    "v_pk_add_f32 v[214:215], v[214:215], v[206:207]\n"
    "v_pk_add_f32 v[212:213], v[212:213], v[204:205]\n"
    "s_waitcnt lgkmcnt(3)\n"
    "v_pk_add_f32 v[214:215], v[214:215], v[22:23]\n"
    "v_pk_add_f32 v[212:213], v[212:213], v[20:21]\n"
    "s_waitcnt lgkmcnt(2)\n"
    "v_pk_add_f32 v[210:211], v[210:211], v[194:195]\n"
    "v_pk_add_f32 v[208:209], v[208:209], v[192:193]\n"
    "v_cmp_le_f32 vcc, v24, v215\n"
    "v_cmp_ge_f32_e64 s[38:39], v25, v215\n"
    "v_rcp_f32 v26, v215\n"
    "s_waitcnt lgkmcnt(1)\n"
    "v_pk_add_f32 v[210:211], v[210:211], v[202:203]\n"
    "v_pk_add_f32 v[208:209], v[208:209], v[200:201]\n"
    "s_waitcnt lgkmcnt(0)\n"
    "v_pk_add_f32 v[210:211], v[210:211], v[18:19]\n"
    "v_pk_add_f32 v[208:209], v[208:209], v[16:17]\n"
    "s_and_b64 s[38:39], s[38:39], vcc\n"
    "s_cmp_lg_u64 s[38:39], -1\n"
    "s_cbranch_scc0 .Lk_if29\n"
    "v_mov_b32 v208, 0\n"
    "v_mov_b32 v209, 0\n"
    "v_mov_b32 v210, 0\n"
    "v_mov_b32 v211, 0\n"
    "v_mov_b32 v212, 0\n"
    "v_mov_b32 v213, 0\n"
    "v_mov_b32 v214, 0\n"
    "v_mov_b32 v215, 0\n"
    "v_mov_b32 v216, 0\n"
    "v_mov_b32 v217, 0\n"
    "v_mov_b32 v218, 0\n"
    "v_mov_b32 v219, 0\n"
    "v_mov_b32 v220, 0\n"
    "v_mov_b32 v221, 0\n"
    "v_mov_b32 v222, 0\n"
    "v_mov_b32 v223, 0\n"
    "v_mov_b32 v16, 0xff7fffff\n"
    "s_and_b32 s47, s61, 0xffff8000\n"
    "s_and_b32 s48, s63, 0xfffc0fff\n"
    "s_mov_b32 s70, 64\n"
    ".Lk_lp28:\n"
    "s_add_u32 s46, s47, 0x10000\n"
    "buffer_load_dwordx4 v[40:43], v245, s[20:23], s47 offen sc1\n"
    "buffer_load_dwordx4 v[44:47], v245, s[20:23], s46 offen sc1\n"
    "s_mov_b64 exec, s[36:37]\n"
    "buffer_load_dwordx4 v[48:51], v254, s[20:23], s48 offen sc1\n"
    "buffer_load_dwordx4 v[52:55], v254, s[20:23], s48 offen offset:2048 sc1\n"
    "s_mov_b64 exec, -1\n"
    "s_add_u32 s47, s47, 0x200\n"
    "s_add_u32 s48, s48, 0x1000\n"
    "s_waitcnt vmcnt(3)\n"
    "v_mfma_f32_32x32x16_bf16 v[0:15], v[40:43], v[232:235], 0\n"
    "s_waitcnt vmcnt(2)\n"
    "v_mfma_f32_32x32x16_bf16 v[0:15], v[44:47], v[236:239], v[0:15]\n"
    "s_nop 11\n"
    "v_max3_f32 v19, v0, v1, v16\n"
    "v_max3_f32 v19, v2, v3, v19\n"
    "v_max3_f32 v19, v4, v5, v19\n"
    "v_max3_f32 v19, v6, v7, v19\n"
    "v_max3_f32 v19, v8, v9, v19\n"
    "v_max3_f32 v19, v10, v11, v19\n"
    "v_max3_f32 v19, v12, v13, v19\n"
    "v_max3_f32 v19, v14, v15, v19\n"
    "v_mov_b32 v20, v19\n"
    "s_nop 1\n"
    "v_permlane32_swap_b32 v19, v20\n"
    "v_max_f32 v17, v19, v20\n"
    "v_sub_f32 v18, v16, v17\n"
    "v_exp_f32 v18, v18\n"
    "v_mov_b32 v16, v17\n"
    "v_sub_f32 v0, v0, v17\n"
    "v_exp_f32 v0, v0\n"
    "v_mul_f32 v208, v208, v18\n"
    "v_sub_f32 v1, v1, v17\n"
    "v_exp_f32 v1, v1\n"
    "v_mul_f32 v209, v209, v18\n"
    "v_sub_f32 v2, v2, v17\n"
    "v_exp_f32 v2, v2\n"
    "v_mul_f32 v210, v210, v18\n"
    "v_sub_f32 v3, v3, v17\n"
    "v_exp_f32 v3, v3\n"
    "v_mul_f32 v211, v211, v18\n"
    "v_sub_f32 v4, v4, v17\n"
    "v_exp_f32 v4, v4\n"
    "v_mul_f32 v212, v212, v18\n"
    "v_sub_f32 v5, v5, v17\n"
    "v_exp_f32 v5, v5\n"
    "v_mul_f32 v213, v213, v18\n"
    "v_sub_f32 v6, v6, v17\n"
    "v_exp_f32 v6, v6\n"
    "v_mul_f32 v214, v214, v18\n"
    "v_sub_f32 v7, v7, v17\n"
    "v_exp_f32 v7, v7\n"
    "v_mul_f32 v215, v215, v18\n"
    "v_sub_f32 v8, v8, v17\n"
    "v_exp_f32 v8, v8\n"
    "v_mul_f32 v216, v216, v18\n"
    "v_sub_f32 v9, v9, v17\n"
    "v_exp_f32 v9, v9\n"
    "v_mul_f32 v217, v217, v18\n"
    "v_sub_f32 v10, v10, v17\n"
    "v_exp_f32 v10, v10\n"
    "v_mul_f32 v218, v218, v18\n"
    "v_sub_f32 v11, v11, v17\n"
    "v_exp_f32 v11, v11\n"
    "v_mul_f32 v219, v219, v18\n"
    "v_sub_f32 v12, v12, v17\n"
    "v_exp_f32 v12, v12\n"
    "v_mul_f32 v220, v220, v18\n"
    "v_sub_f32 v13, v13, v17\n"
    "v_exp_f32 v13, v13\n"
    "v_mul_f32 v221, v221, v18\n"
    "v_sub_f32 v14, v14, v17\n"
    "v_exp_f32 v14, v14\n"
    "v_mul_f32 v222, v222, v18\n"
    "v_sub_f32 v15, v15, v17\n"
    "v_exp_f32 v15, v15\n"
    "v_mul_f32 v223, v223, v18\n"
    "v_cvt_pk_bf16_f32 v96, v0, v1\n"
    "v_cvt_pk_bf16_f32 v97, v2, v3\n"
    "v_cvt_pk_bf16_f32 v98, v4, v5\n"
    "v_cvt_pk_bf16_f32 v99, v6, v7\n"
    "v_cvt_pk_bf16_f32 v100, v8, v9\n"
    "v_cvt_pk_bf16_f32 v101, v10, v11\n"
    "v_cvt_pk_bf16_f32 v102, v12, v13\n"
    "v_cvt_pk_bf16_f32 v103, v14, v15\n"
    "s_waitcnt vmcnt(1)\n"
    "v_mfma_f32_32x32x16_bf16 v[208:223], v[48:51], v[96:99], v[208:223]\n"
    "s_waitcnt vmcnt(0)\n"
    "v_mfma_f32_32x32x16_bf16 v[208:223], v[52:55], v[100:103], v[208:223]\n"
    "s_sub_u32 s70, s70, 1\n"
    "s_cmp_lg_u32 s70, 0\n"
    "s_cbranch_scc1 .Lk_lp28\n"
    "s_nop 11\n"
    "v_rcp_f32 v26, v215\n"
    "s_nop 0\n"
    ".Lk_if29:\n"
    "v_pk_mul_f32 v[214:215], v[214:215], v[26:27] op_sel_hi:[1,0]\n"
    "v_pk_mul_f32 v[208:209], v[208:209], v[26:27] op_sel_hi:[1,0]\n"
    "v_pk_mul_f32 v[210:211], v[210:211], v[26:27] op_sel_hi:[1,0]\n"
    "v_pk_mul_f32 v[212:213], v[212:213], v[26:27] op_sel_hi:[1,0]\n"
    "v_cvt_pk_bf16_f32 v214, v214, 0\n"
    "v_cvt_pk_bf16_f32 v208, v208, v209\n"
    "v_cvt_pk_bf16_f32 v209, v210, v211\n"
    "v_cvt_pk_bf16_f32 v210, v212, v213\n"
    "v_lshl_or_b32 v211, v240, 16, v214\n"
    "buffer_store_dwordx4 v[208:211], v244, s[20:23], s67 offen sc1\n"
    "s_cmp_eq_u32 s26, 0\n"
    "s_cbranch_scc0 .Lk_if35\n"
    "s_lshl3_add_u32 s45, %2, s40\n"
    "s_cmp_eq_u32 s45, 0x7f8\n"
    "s_cbranch_scc0 .Lk_if34\n"
    "v_lshlrev_b32 v2, 3, v244\n"
    "s_mov_b32 s70, 0x40000\n"
    ".Lk_lp30:\n"
    "s_sleep 1\n"
    "s_mov_b32 s46, 0xf00000\n"
    "buffer_load_dwordx2 v[242:243], v2, s[20:23], s46 offen sc1\n"
    "s_waitcnt vmcnt(0)\n"
    "v_cmp_ne_u64 vcc, v[242:243], v[240:241]\n"
    "s_sub_u32 s70, s70, 1\n"
    "s_nop 0\n"
    "s_cselect_b64 s[38:39], 0, vcc\n"
    "s_cmp_lg_u64 s[38:39], 0\n"
    "s_cbranch_scc1 .Lk_lp30\n"
    "s_mov_b32 s70, 0x40000\n"
    ".Lk_lp31:\n"
    "s_sleep 1\n"
    "s_mov_b32 s46, 0xf02000\n"
    "buffer_load_dwordx2 v[242:243], v2, s[20:23], s46 offen sc1\n"
    "s_waitcnt vmcnt(0)\n"
    "v_cmp_ne_u64 vcc, v[242:243], v[240:241]\n"
    "s_sub_u32 s70, s70, 1\n"
    "s_nop 0\n"
    "s_cselect_b64 s[38:39], 0, vcc\n"
    "s_cmp_lg_u64 s[38:39], 0\n"
    "s_cbranch_scc1 .Lk_lp31\n"
    "s_mov_b32 s70, 0x40000\n"
    ".Lk_lp32:\n"
    "s_sleep 1\n"
    "s_mov_b32 s46, 0xf04000\n"
    "buffer_load_dwordx2 v[242:243], v2, s[20:23], s46 offen sc1\n"
    "s_waitcnt vmcnt(0)\n"
    "v_cmp_ne_u64 vcc, v[242:243], v[240:241]\n"
    "s_sub_u32 s70, s70, 1\n"
    "s_nop 0\n"
    "s_cselect_b64 s[38:39], 0, vcc\n"
    "s_cmp_lg_u64 s[38:39], 0\n"
    "s_cbranch_scc1 .Lk_lp32\n"
    "s_mov_b32 s70, 0x40000\n"
    ".Lk_lp33:\n"
    "s_sleep 1\n"
    "s_mov_b32 s46, 0xf06000\n"
    "buffer_load_dwordx2 v[242:243], v2, s[20:23], s46 offen sc1\n"
    "s_waitcnt vmcnt(0)\n"
    "v_cmp_ne_u64 vcc, v[242:243], v[240:241]\n"
    "s_sub_u32 s70, s70, 1\n"
    "s_nop 0\n"
    "s_cselect_b64 s[38:39], 0, vcc\n"
    "s_cmp_lg_u64 s[38:39], 0\n"
    "s_cbranch_scc1 .Lk_lp33\n"
    "s_mov_b64 exec, 1\n"
    "s_mov_b32 s45, 0xf10000\n"
    "buffer_store_dwordx2 v[240:241], off, s[20:23], s45 sc1\n"
    "s_mov_b64 exec, -1\n"
    ".Lk_if34:\n"
    "s_endpgm\n"
    ".Lk_if35:\n"
    "s_add_u32 s46, s68, 0x1000\n"
    "ds_read_b64_tr_b16 v[112:113], v252 offset:49152\n"
    "ds_read_b64_tr_b16 v[114:115], v252 offset:50176\n"
    "ds_read_b64_tr_b16 v[116:117], v252 offset:32768\n"
    "ds_read_b64_tr_b16 v[118:119], v252 offset:33792\n"
    "ds_read_b64_tr_b16 v[120:121], v252 offset:57344\n"
    "ds_read_b64_tr_b16 v[122:123], v252 offset:58368\n"
    "ds_read_b64_tr_b16 v[124:125], v251 offset:57344\n"
    "ds_read_b64_tr_b16 v[126:127], v251 offset:58368\n"
    "v_mfma_f32_32x32x16_bf16 v[80:95], v[104:107], v[156:159], v[80:95]\n"
    "v_add_u32 v251, 0x16000, v251\n"
    "ds_read_b64_tr_b16 v[104:105], v251 offset:40960\n"
    "ds_read_b64_tr_b16 v[106:107], v251 offset:41984\n"
    "v_xor_b32 v247, 0x1000, v255\n"
    "v_add_u32 v250, 0x20000, v255\n"
    "v_lshrrev_b32 v41, 9, v244\n"
    "v_bfe_u32 v40, v244, 6, 2\n"
    "v_and_b32 v42, 0x138, v251\n"
    "v_mfma_f32_32x32x16_bf16 v[80:95], v[108:111], v[128:131], v[80:95]\n"
    "ds_read_b64_tr_b16 v[108:109], v251 offset:49152\n"
    "ds_read_b64_tr_b16 v[110:111], v251 offset:50176\n"
    "s_lshr_b32 s47, s40, 1\n"
    "s_mul_i32 s48, s47, 0x3800\n"
    "s_add_u32 s48, s48, 0x12000\n"
    "v_cmp_gt_u32 vcc, 3, v40\n"
    "v_mad_u32_u24 v41, v41, 7, v40\n"
    "v_add_u32 v42, s48, v42\n"
    "s_waitcnt lgkmcnt(10)\n"
    "v_mfma_f32_32x32x16_bf16 v[80:95], v[112:115], v[132:135], v[80:95]\n"
    "ds_read_b64_tr_b16 v[112:113], v251 offset:32768\n"
    "ds_read_b64_tr_b16 v[114:115], v251 offset:33792\n"
    "v_mov_b32 v40, 0x200\n"
    "v_cndmask_b32 v40, 0, v40, vcc\n"
    "v_lshrrev_b32 v43, 2, v41\n"
    "v_lshl_add_u32 v96, v43, 9, v42\n"
    "v_and_b32 v43, 3, v41\n"
    "s_waitcnt lgkmcnt(10)\n"
    "v_mfma_f32_32x32x16_bf16 v[80:95], v[116:119], v[136:139], v[80:95]\n"
    "ds_read_b64_tr_b16 v[116:117], v251 offset:57344\n"
    "ds_read_b64_tr_b16 v[118:119], v251 offset:58368\n"
    "v_lshl_add_u32 v96, v43, 6, v96\n"
    "v_add_u32 v97, v40, v96\n"
    "v_add_u32 v41, 14, v41\n"
    "v_lshrrev_b32 v43, 2, v41\n"
    "v_lshl_add_u32 v98, v43, 9, v42\n"
    "s_waitcnt lgkmcnt(10)\n"
    "v_mfma_f32_32x32x16_bf16 v[80:95], v[120:123], v[140:143], v[80:95]\n"
    "v_and_b32 v43, 3, v41\n"
    "v_lshl_add_u32 v98, v43, 6, v98\n"
    "v_add_u32 v99, v40, v98\n"
    "v_xor_b32 v100, 0x100, v96\n"
    "v_xor_b32 v101, 0x100, v97\n"
    "s_waitcnt lgkmcnt(8)\n"
    "v_mfma_f32_32x32x16_bf16 v[64:79], v[124:127], v[156:159], v[64:79]\n"
    "v_xor_b32 v102, 0x100, v98\n"
    "v_xor_b32 v103, 0x100, v99\n"
    "v_mov_b32 v40, 0xffff\n"
    "v_lshlrev_b32 v41, 16, v240\n"
    "s_waitcnt vmcnt(1)\n"
    "v_lshlrev_b32 v144, 16, v152\n"
    "s_waitcnt lgkmcnt(6)\n"
    "v_mfma_f32_32x32x16_bf16 v[64:79], v[104:107], v[128:131], v[64:79]\n"
    "v_and_b32 v145, s71, v152\n"
    "v_lshlrev_b32 v146, 16, v153\n"
    "v_and_b32 v147, s71, v153\n"
    "v_lshlrev_b32 v148, 16, v154\n"
    "v_and_b32 v149, s71, v154\n"
    "ds_write_b128 v248, v[80:83] offset:32768\n"
    "s_waitcnt lgkmcnt(5)\n"
    "v_mfma_f32_32x32x16_bf16 v[64:79], v[108:111], v[132:135], v[64:79]\n"
    "v_lshlrev_b32 v150, 16, v155\n"
    "v_and_b32 v151, s71, v155\n"
    "ds_write_b128 v248, v[84:87] offset:33792\n"
    "s_waitcnt lgkmcnt(4)\n"
    "v_mfma_f32_32x32x16_bf16 v[64:79], v[112:115], v[136:139], v[64:79]\n"
    "ds_write_b128 v246, v[88:91]\n"
    "s_waitcnt lgkmcnt(3)\n"
    "v_mfma_f32_32x32x16_bf16 v[64:79], v[116:119], v[140:143], v[64:79]\n"
    "ds_write_b128 v246, v[92:95] offset:1024\n"
    "ds_read_b64_tr_b16 v[164:165], v100\n"
    "ds_read_b64_tr_b16 v[166:167], v101\n"
    "ds_read_b64_tr_b16 v[160:161], v96\n"
    "ds_read_b64_tr_b16 v[162:163], v97\n"
    "s_nop 6\n"
    "ds_write_b128 v247, v[72:75] offset:49152\n"
    "ds_write_b128 v247, v[76:79] offset:50176\n"
    "buffer_load_dwordx4 v[224:227], v244, s[20:23], s68 offen sc1\n"
    "buffer_load_dwordx4 v[228:231], v244, s[20:23], s68 offen offset:1024 sc1\n"
    "buffer_load_dwordx4 v[232:235], v244, s[20:23], s68 offen offset:2048 sc1\n"
    "buffer_load_dwordx4 v[236:239], v244, s[20:23], s68 offen offset:3072 sc1\n"
    "ds_read_b64_tr_b16 v[172:173], v102\n"
    "ds_read_b64_tr_b16 v[174:175], v103\n"
    "ds_read_b64_tr_b16 v[168:169], v98\n"
    "ds_read_b64_tr_b16 v[170:171], v99\n"
    "ds_read_b64_tr_b16 v[180:181], v100 offset:3584\n"
    "ds_read_b64_tr_b16 v[182:183], v101 offset:3584\n"
    "ds_read_b64_tr_b16 v[176:177], v96 offset:3584\n"
    "ds_read_b64_tr_b16 v[178:179], v97 offset:3584\n"
    "v_xor_b32 v248, 0x2000, v250\n"
    "v_xor_b32 v246, 0x2800, v255\n"
    "s_waitcnt lgkmcnt(8)\n"
    "s_barrier\n"
    "ds_read_b128 v[104:107], v255 offset:49152\n"
    "ds_read_b128 v[108:111], v255 offset:50176\n"
    "ds_read_b128 v[112:115], v255 offset:32768\n"
    "ds_read_b128 v[116:119], v255 offset:33792\n"
    "ds_read_b128 v[120:123], v246\n"
    "ds_read_b128 v[124:127], v246 offset:1024\n"
    "buffer_load_dwordx4 v[48:51], v244, s[20:23], s46 offen sc1\n"
    "buffer_load_dwordx4 v[52:55], v244, s[20:23], s46 offen offset:1024 sc1\n"
    "buffer_load_dwordx4 v[56:59], v244, s[20:23], s46 offen offset:2048 sc1\n"
    "buffer_load_dwordx4 v[60:63], v244, s[20:23], s46 offen offset:3072 sc1\n"
    "v_xor_b32 v246, 0x3000, v250\n"
    "s_waitcnt lgkmcnt(5)\n"
    "v_add_f32 v64, v64, v104\n"
    "s_waitcnt lgkmcnt(1)\n"
    "v_add_f32 v112, v112, v120\n"
    "v_add_f32 v65, v65, v105\n"
    "v_add_f32 v113, v113, v121\n"
    "v_add_f32 v66, v66, v106\n"
    "v_add_f32 v114, v114, v122\n"
    "v_add_f32 v67, v67, v107\n"
    "v_add_f32 v115, v115, v123\n"
    "v_add_f32 v68, v68, v108\n"
    "s_waitcnt lgkmcnt(0)\n"
    "v_add_f32 v116, v116, v124\n"
    "v_add_f32 v69, v69, v109\n"
    "v_add_f32 v117, v117, v125\n"
    "v_add_f32 v70, v70, v110\n"
    "v_add_f32 v118, v118, v126\n"
    "v_add_f32 v71, v71, v111\n"
    "v_add_f32 v119, v119, v127\n"
    "v_add_f32 v64, v64, v112\n"
    "v_add_f32 v65, v65, v113\n"
    "v_add_f32 v66, v66, v114\n"
    "v_add_f32 v67, v67, v115\n"
    "v_add_f32 v68, v68, v116\n"
    "v_add_f32 v69, v69, v117\n"
    "v_add_f32 v70, v70, v118\n"
    "v_add_f32 v71, v71, v119\n"
    "v_mul_f32 v72, 0xbfb8aa3b, v64\n"
    "v_mul_f32 v73, 0xbfb8aa3b, v65\n"
    "v_mul_f32 v74, 0xbfb8aa3b, v66\n"
    "v_mul_f32 v75, 0xbfb8aa3b, v67\n"
    "v_mul_f32 v76, 0xbfb8aa3b, v68\n"
    "v_mul_f32 v77, 0xbfb8aa3b, v69\n"
    "v_mul_f32 v78, 0xbfb8aa3b, v70\n"
    "v_mul_f32 v79, 0xbfb8aa3b, v71\n"
    "v_exp_f32 v72, v72\n"
    "v_exp_f32 v73, v73\n"
    "v_exp_f32 v74, v74\n"
    "v_exp_f32 v75, v75\n"
    "s_waitcnt vmcnt(7)\n"
    "v_xor_b32 v227, v41, v227\n"
    "v_cmp_lt_u32 vcc, v40, v227\n"
    "v_exp_f32 v76, v76\n"
    "s_nop 0\n"
    "s_cmp_lg_u64 vcc, 0\n"
    "s_cbranch_scc0 .Lk_if37\n"
    "s_mov_b32 s70, 0x10000\n"
    ".Lk_lp36:\n"
    "s_add_u32 s46, s68, 0x1000\n"
    "buffer_load_dwordx4 v[224:227], v244, s[20:23], s68 offen sc1\n"
    "s_waitcnt vmcnt(7)\n"
    "buffer_load_dwordx4 v[228:231], v244, s[20:23], s68 offen offset:1024 sc1\n"
    "s_waitcnt vmcnt(7)\n"
    "buffer_load_dwordx4 v[232:235], v244, s[20:23], s68 offen offset:2048 sc1\n"
    "s_waitcnt vmcnt(7)\n"
    "buffer_load_dwordx4 v[236:239], v244, s[20:23], s68 offen offset:3072 sc1\n"
    "s_waitcnt vmcnt(7)\n"
    "buffer_load_dwordx4 v[48:51], v244, s[20:23], s46 offen sc1\n"
    "s_waitcnt vmcnt(7)\n"
    "buffer_load_dwordx4 v[52:55], v244, s[20:23], s46 offen offset:1024 sc1\n"
    "s_waitcnt vmcnt(7)\n"
    "buffer_load_dwordx4 v[56:59], v244, s[20:23], s46 offen offset:2048 sc1\n"
    "s_waitcnt vmcnt(7)\n"
    "buffer_load_dwordx4 v[60:63], v244, s[20:23], s46 offen offset:3072 sc1\n"
    "s_waitcnt vmcnt(7)\n"
    "v_xor_b32 v227, v41, v227\n"
    "v_cmp_lt_u32 vcc, v40, v227\n"
    "s_sub_u32 s70, s70, 1\n"
    "s_nop 0\n"
    "s_cselect_b64 s[38:39], 0, vcc\n"
    "s_cmp_lg_u64 s[38:39], 0\n"
    "s_cbranch_scc1 .Lk_lp36\n"
    ".Lk_if37:\n"
    "v_mfma_f32_32x32x16_bf16 v[16:31], v[164:167], v[224:227], 0\n"
    "ds_read_b64_tr_b16 v[188:189], v102 offset:3584\n"
    "ds_read_b64_tr_b16 v[190:191], v103 offset:3584\n"
    "v_exp_f32 v77, v77\n"
    "ds_read_b64_tr_b16 v[184:185], v98 offset:3584\n"
    "ds_read_b64_tr_b16 v[186:187], v99 offset:3584\n"
    "v_exp_f32 v78, v78\n"
    "v_exp_f32 v79, v79\n"
    "s_waitcnt vmcnt(6)\n"
    "v_xor_b32 v231, v41, v231\n"
    "v_cmp_lt_u32 vcc, v40, v231\n"
    "s_nop 1\n"
    "s_cmp_lg_u64 vcc, 0\n"
    "s_cbranch_scc0 .Lk_if39\n"
    "s_mov_b32 s70, 0x10000\n"
    ".Lk_lp38:\n"
    "s_add_u32 s46, s68, 0x1000\n"
    "buffer_load_dwordx4 v[228:231], v244, s[20:23], s68 offen offset:1024 sc1\n"
    "s_waitcnt vmcnt(6)\n"
    "buffer_load_dwordx4 v[232:235], v244, s[20:23], s68 offen offset:2048 sc1\n"
    "s_waitcnt vmcnt(6)\n"
    "buffer_load_dwordx4 v[236:239], v244, s[20:23], s68 offen offset:3072 sc1\n"
    "s_waitcnt vmcnt(6)\n"
    "buffer_load_dwordx4 v[48:51], v244, s[20:23], s46 offen sc1\n"
    "s_waitcnt vmcnt(6)\n"
    "buffer_load_dwordx4 v[52:55], v244, s[20:23], s46 offen offset:1024 sc1\n"
    "s_waitcnt vmcnt(6)\n"
    "buffer_load_dwordx4 v[56:59], v244, s[20:23], s46 offen offset:2048 sc1\n"
    "s_waitcnt vmcnt(6)\n"
    "buffer_load_dwordx4 v[60:63], v244, s[20:23], s46 offen offset:3072 sc1\n"
    "s_waitcnt vmcnt(6)\n"
    "v_xor_b32 v231, v41, v231\n"
    "v_cmp_lt_u32 vcc, v40, v231\n"
    "s_sub_u32 s70, s70, 1\n"
    "s_nop 0\n"
    "s_cselect_b64 s[38:39], 0, vcc\n"
    "s_cmp_lg_u64 s[38:39], 0\n"
    "s_cbranch_scc1 .Lk_lp38\n"
    ".Lk_if39:\n"
    "v_mfma_f32_32x32x16_bf16 v[16:31], v[172:175], v[228:231], v[16:31]\n"
    "ds_read_b64_tr_b16 v[196:197], v100 offset:7168\n"
    "ds_read_b64_tr_b16 v[198:199], v101 offset:7168\n"
    "v_add_f32 v72, 1.0, v72\n"
    "v_add_f32 v73, 1.0, v73\n"
    "v_add_f32 v74, 1.0, v74\n"
    "ds_read_b64_tr_b16 v[192:193], v96 offset:7168\n"
    "ds_read_b64_tr_b16 v[194:195], v97 offset:7168\n"
    "v_add_f32 v75, 1.0, v75\n"
    "v_add_f32 v76, 1.0, v76\n"
    "v_add_f32 v77, 1.0, v77\n"
    "s_waitcnt vmcnt(5)\n"
    "v_xor_b32 v235, v41, v235\n"
    "v_cmp_lt_u32 vcc, v40, v235\n"
    "v_add_f32 v78, 1.0, v78\n"
    "s_nop 0\n"
    "s_cmp_lg_u64 vcc, 0\n"
    "s_cbranch_scc0 .Lk_if41\n"
    "s_mov_b32 s70, 0x10000\n"
    ".Lk_lp40:\n"
    "s_add_u32 s46, s68, 0x1000\n"
    "buffer_load_dwordx4 v[232:235], v244, s[20:23], s68 offen offset:2048 sc1\n"
    "s_waitcnt vmcnt(5)\n"
    "buffer_load_dwordx4 v[236:239], v244, s[20:23], s68 offen offset:3072 sc1\n"
    "s_waitcnt vmcnt(5)\n"
    "buffer_load_dwordx4 v[48:51], v244, s[20:23], s46 offen sc1\n"
    "s_waitcnt vmcnt(5)\n"
    "buffer_load_dwordx4 v[52:55], v244, s[20:23], s46 offen offset:1024 sc1\n"
    "s_waitcnt vmcnt(5)\n"
    "buffer_load_dwordx4 v[56:59], v244, s[20:23], s46 offen offset:2048 sc1\n"
    "s_waitcnt vmcnt(5)\n"
    "buffer_load_dwordx4 v[60:63], v244, s[20:23], s46 offen offset:3072 sc1\n"
    "s_waitcnt vmcnt(5)\n"
    "v_xor_b32 v235, v41, v235\n"
    "v_cmp_lt_u32 vcc, v40, v235\n"
    "s_sub_u32 s70, s70, 1\n"
    "s_nop 0\n"
    "s_cselect_b64 s[38:39], 0, vcc\n"
    "s_cmp_lg_u64 s[38:39], 0\n"
    "s_cbranch_scc1 .Lk_lp40\n"
    ".Lk_if41:\n"
    "v_mfma_f32_32x32x16_bf16 v[16:31], v[180:183], v[232:235], v[16:31]\n"
    "ds_read_b64_tr_b16 v[204:205], v102 offset:7168\n"
    "ds_read_b64_tr_b16 v[206:207], v103 offset:7168\n"
    "v_add_f32 v79, 1.0, v79\n"
    "v_rcp_f32 v72, v72\n"
    "v_mfma_f32_32x32x16_bf16 v[0:15], v[160:163], v[224:227], 0\n"
    "ds_read_b64_tr_b16 v[200:201], v98 offset:7168\n"
    "ds_read_b64_tr_b16 v[202:203], v99 offset:7168\n"
    "v_rcp_f32 v73, v73\n"
    "v_rcp_f32 v74, v74\n"
    "s_waitcnt vmcnt(4)\n"
    "v_xor_b32 v239, v41, v239\n"
    "v_cmp_lt_u32 vcc, v40, v239\n"
    "s_nop 1\n"
    "s_cmp_lg_u64 vcc, 0\n"
    "s_cbranch_scc0 .Lk_if43\n"
    "s_mov_b32 s70, 0x10000\n"
    ".Lk_lp42:\n"
    "s_add_u32 s46, s68, 0x1000\n"
    "buffer_load_dwordx4 v[236:239], v244, s[20:23], s68 offen offset:3072 sc1\n"
    "s_waitcnt vmcnt(4)\n"
    "buffer_load_dwordx4 v[48:51], v244, s[20:23], s46 offen sc1\n"
    "s_waitcnt vmcnt(4)\n"
    "buffer_load_dwordx4 v[52:55], v244, s[20:23], s46 offen offset:1024 sc1\n"
    "s_waitcnt vmcnt(4)\n"
    "buffer_load_dwordx4 v[56:59], v244, s[20:23], s46 offen offset:2048 sc1\n"
    "s_waitcnt vmcnt(4)\n"
    "buffer_load_dwordx4 v[60:63], v244, s[20:23], s46 offen offset:3072 sc1\n"
    "s_waitcnt vmcnt(4)\n"
    "v_xor_b32 v239, v41, v239\n"
    "v_cmp_lt_u32 vcc, v40, v239\n"
    "s_sub_u32 s70, s70, 1\n"
    "s_nop 0\n"
    "s_cselect_b64 s[38:39], 0, vcc\n"
    "s_cmp_lg_u64 s[38:39], 0\n"
    "s_cbranch_scc1 .Lk_lp42\n"
    ".Lk_if43:\n"
    "s_waitcnt lgkmcnt(10)\n"
    "v_mfma_f32_32x32x16_bf16 v[16:31], v[188:191], v[236:239], v[16:31]\n"
    "ds_read_b64_tr_b16 v[212:213], v100 offset:10752\n"
    "ds_read_b64_tr_b16 v[214:215], v101 offset:10752\n"
    "v_rcp_f32 v75, v75\n"
    "v_rcp_f32 v76, v76\n"
    "v_mfma_f32_32x32x16_bf16 v[0:15], v[168:171], v[228:231], v[0:15]\n"
    "ds_read_b64_tr_b16 v[208:209], v96 offset:10752\n"
    "ds_read_b64_tr_b16 v[210:211], v97 offset:10752\n"
    "v_rcp_f32 v77, v77\n"
    "s_waitcnt vmcnt(3)\n"
    "v_xor_b32 v51, v41, v51\n"
    "v_cmp_lt_u32 vcc, v40, v51\n"
    "v_rcp_f32 v78, v78\n"
    "s_nop 0\n"
    "s_cmp_lg_u64 vcc, 0\n"
    "s_cbranch_scc0 .Lk_if45\n"
    "s_mov_b32 s70, 0x10000\n"
    ".Lk_lp44:\n"
    "s_add_u32 s46, s68, 0x1000\n"
    "buffer_load_dwordx4 v[48:51], v244, s[20:23], s46 offen sc1\n"
    "s_waitcnt vmcnt(3)\n"
    "buffer_load_dwordx4 v[52:55], v244, s[20:23], s46 offen offset:1024 sc1\n"
    "s_waitcnt vmcnt(3)\n"
    "buffer_load_dwordx4 v[56:59], v244, s[20:23], s46 offen offset:2048 sc1\n"
    "s_waitcnt vmcnt(3)\n"
    "buffer_load_dwordx4 v[60:63], v244, s[20:23], s46 offen offset:3072 sc1\n"
    "s_waitcnt vmcnt(3)\n"
    "v_xor_b32 v51, v41, v51\n"
    "v_cmp_lt_u32 vcc, v40, v51\n"
    "s_sub_u32 s70, s70, 1\n"
    "s_nop 0\n"
    "s_cselect_b64 s[38:39], 0, vcc\n"
    "s_cmp_lg_u64 s[38:39], 0\n"
    "s_cbranch_scc1 .Lk_lp44\n"
    ".Lk_if45:\n"
    "s_waitcnt lgkmcnt(10)\n"
    "v_mfma_f32_32x32x16_bf16 v[16:31], v[196:199], v[48:51], v[16:31]\n"
    "ds_read_b64_tr_b16 v[220:221], v102 offset:10752\n"
    "ds_read_b64_tr_b16 v[222:223], v103 offset:10752\n"
    "v_rcp_f32 v79, v79\n"
    "v_mfma_f32_32x32x16_bf16 v[0:15], v[176:179], v[232:235], v[0:15]\n"
    "ds_read_b64_tr_b16 v[216:217], v98 offset:10752\n"
    "ds_read_b64_tr_b16 v[218:219], v99 offset:10752\n"
    "v_mul_f32 v64, v64, v72\n"
    "v_mul_f32 v65, v65, v73\n"
    "v_mul_f32 v66, v66, v74\n"
    "s_waitcnt vmcnt(2)\n"
    "v_xor_b32 v55, v41, v55\n"
    "v_cmp_lt_u32 vcc, v40, v55\n"
    "v_mul_f32 v67, v67, v75\n"
    "s_nop 0\n"
    "s_cmp_lg_u64 vcc, 0\n"
    "s_cbranch_scc0 .Lk_if47\n"
    "s_mov_b32 s70, 0x10000\n"
    ".Lk_lp46:\n"
    "s_add_u32 s46, s68, 0x1000\n"
    "buffer_load_dwordx4 v[52:55], v244, s[20:23], s46 offen offset:1024 sc1\n"
    "s_waitcnt vmcnt(2)\n"
    "buffer_load_dwordx4 v[56:59], v244, s[20:23], s46 offen offset:2048 sc1\n"
    "s_waitcnt vmcnt(2)\n"
    "buffer_load_dwordx4 v[60:63], v244, s[20:23], s46 offen offset:3072 sc1\n"
    "s_waitcnt vmcnt(2)\n"
    "v_xor_b32 v55, v41, v55\n"
    "v_cmp_lt_u32 vcc, v40, v55\n"
    "s_sub_u32 s70, s70, 1\n"
    "s_nop 0\n"
    "s_cselect_b64 s[38:39], 0, vcc\n"
    "s_cmp_lg_u64 s[38:39], 0\n"
    "s_cbranch_scc1 .Lk_lp46\n"
    ".Lk_if47:\n"
    "s_waitcnt lgkmcnt(10)\n"
    "v_mfma_f32_32x32x16_bf16 v[16:31], v[204:207], v[52:55], v[16:31]\n"
    "v_mul_f32 v68, v68, v76\n"
    "v_mul_f32 v69, v69, v77\n"
    "v_mul_f32 v70, v70, v78\n"
    "v_mfma_f32_32x32x16_bf16 v[0:15], v[184:187], v[236:239], v[0:15]\n"
    "v_mul_f32 v71, v71, v79\n"
    "s_waitcnt vmcnt(1)\n"
    "v_xor_b32 v59, v41, v59\n"
    "v_cmp_lt_u32 vcc, v40, v59\n"
    "s_nop 1\n"
    "s_cmp_lg_u64 vcc, 0\n"
    "s_cbranch_scc0 .Lk_if49\n"
    "s_mov_b32 s70, 0x10000\n"
    ".Lk_lp48:\n"
    "s_add_u32 s46, s68, 0x1000\n"
    "buffer_load_dwordx4 v[56:59], v244, s[20:23], s46 offen offset:2048 sc1\n"
    "s_waitcnt vmcnt(1)\n"
    "buffer_load_dwordx4 v[60:63], v244, s[20:23], s46 offen offset:3072 sc1\n"
    "s_waitcnt vmcnt(1)\n"
    "v_xor_b32 v59, v41, v59\n"
    "v_cmp_lt_u32 vcc, v40, v59\n"
    "s_sub_u32 s70, s70, 1\n"
    "s_nop 0\n"
    "s_cselect_b64 s[38:39], 0, vcc\n"
    "s_cmp_lg_u64 s[38:39], 0\n"
    "s_cbranch_scc1 .Lk_lp48\n"
    ".Lk_if49:\n"
    "s_waitcnt lgkmcnt(6)\n"
    "v_mfma_f32_32x32x16_bf16 v[16:31], v[212:215], v[56:59], v[16:31]\n"
    "v_mfma_f32_32x32x16_bf16 v[0:15], v[192:195], v[48:51], v[0:15]\n"
    "s_waitcnt vmcnt(0)\n"
    "v_xor_b32 v63, v41, v63\n"
    "v_cmp_lt_u32 vcc, v40, v63\n"
    "s_nop 1\n"
    "s_cmp_lg_u64 vcc, 0\n"
    "s_cbranch_scc0 .Lk_if51\n"
    "s_mov_b32 s70, 0x10000\n"
    ".Lk_lp50:\n"
    "s_add_u32 s46, s68, 0x1000\n"
    "buffer_load_dwordx4 v[60:63], v244, s[20:23], s46 offen offset:3072 sc1\n"
    "s_waitcnt vmcnt(0)\n"
    "v_xor_b32 v63, v41, v63\n"
    "v_cmp_lt_u32 vcc, v40, v63\n"
    "s_sub_u32 s70, s70, 1\n"
    "s_nop 0\n"
    "s_cselect_b64 s[38:39], 0, vcc\n"
    "s_cmp_lg_u64 s[38:39], 0\n"
    "s_cbranch_scc1 .Lk_lp50\n"
    ".Lk_if51:\n"
    "s_waitcnt lgkmcnt(2)\n"
    "v_mfma_f32_32x32x16_bf16 v[16:31], v[220:223], v[60:63], v[16:31]\n"
    "v_mfma_f32_32x32x16_bf16 v[0:15], v[200:203], v[52:55], v[0:15]\n"
    "v_mfma_f32_32x32x16_bf16 v[0:15], v[208:211], v[56:59], v[0:15]\n"
    "s_waitcnt lgkmcnt(0)\n"
    "v_mfma_f32_32x32x16_bf16 v[0:15], v[216:219], v[60:63], v[0:15]\n"
    "s_nop 7\n"
    "ds_write_b128 v248, v[16:19]\n"
    "ds_write_b128 v248, v[20:23] offset:1024\n"
    "ds_write_b128 v246, v[24:27] offset:16384\n"
    "ds_write_b128 v246, v[28:31] offset:17408\n"
    "ds_write_b128 v247, v[8:11] offset:16384\n"
    "ds_write_b128 v247, v[12:15] offset:17408\n"
    "s_waitcnt lgkmcnt(0)\n"
    "s_barrier\n"
    "ds_read_b128 v[104:107], v255 offset:16384\n"
    "ds_read_b128 v[112:115], v250\n"
    "ds_read_b128 v[120:123], v250 offset:16384\n"
    "ds_read_b128 v[108:111], v255 offset:17408\n"
    "ds_read_b128 v[116:119], v250 offset:1024\n"
    "ds_read_b128 v[124:127], v250 offset:17408\n"
    "s_waitcnt lgkmcnt(5)\n"
    "v_pk_add_f32 v[0:1], v[0:1], v[104:105]\n"
    "v_pk_add_f32 v[2:3], v[2:3], v[106:107]\n"
    "s_waitcnt lgkmcnt(3)\n"
    "v_pk_add_f32 v[112:113], v[112:113], v[120:121]\n"
    "v_pk_add_f32 v[114:115], v[114:115], v[122:123]\n"
    "v_pk_add_f32 v[0:1], v[0:1], v[112:113]\n"
    "v_pk_add_f32 v[2:3], v[2:3], v[114:115]\n"
    "v_pk_fma_f32 v[0:1], v[0:1], v[64:65], v[144:145]\n"
    "v_pk_fma_f32 v[2:3], v[2:3], v[66:67], v[146:147]\n"
    "v_cvt_pk_bf16_f32 v0, v0, v1\n"
    "v_cvt_pk_bf16_f32 v1, v2, v3\n"
    "s_waitcnt lgkmcnt(2)\n"
    "v_pk_add_f32 v[4:5], v[4:5], v[108:109]\n"
    "v_pk_add_f32 v[6:7], v[6:7], v[110:111]\n"
    "s_waitcnt lgkmcnt(0)\n"
    "v_pk_add_f32 v[116:117], v[116:117], v[124:125]\n"
    "v_pk_add_f32 v[118:119], v[118:119], v[126:127]\n"
    "v_pk_add_f32 v[4:5], v[4:5], v[116:117]\n"
    "v_pk_add_f32 v[6:7], v[6:7], v[118:119]\n"
    "v_pk_fma_f32 v[4:5], v[4:5], v[68:69], v[148:149]\n"
    "v_pk_fma_f32 v[6:7], v[6:7], v[70:71], v[150:151]\n"
    "v_cvt_pk_bf16_f32 v2, v4, v5\n"
    "v_cvt_pk_bf16_f32 v3, v6, v7\n"
    "buffer_store_dwordx4 v[0:3], v253, s[32:35], s69 offen nt\n"
    "s_endpgm\n"
    ".Lk_end:\n"
    : "+v"(tid) : "s"(__builtin_amdgcn_kernarg_segment_ptr()), "s"(blockIdx.x)
    : "v1", "v2", "v3", "v4", "v5", "v6", "v7", "v8", "v9", "v10", "v11", "v12", "v13", "v14", "v15", "v16", "v17", "v18", "v19", "v20", "v21", "v22", "v23", "v24", "v25", "v26", "v27", "v28", "v29", "v30", "v31", "v32", "v33", "v34", "v35", "v36", "v37", "v38", "v39", "v40", "v41", "v42", "v43", "v44", "v45", "v46", "v47", "v48", "v49", "v50", "v51", "v52", "v53", "v54", "v55", "v56", "v57", "v58", "v59", "v60", "v61", "v62", "v63", "v64", "v65", "v66", "v67", "v68", "v69", "v70", "v71", "v72", "v73", "v74", "v75", "v76", "v77", "v78", "v79", "v80", "v81", "v82", "v83", "v84", "v85", "v86", "v87", "v88", "v89", "v90", "v91", "v92", "v93", "v94", "v95", "v96", "v97", "v98", "v99", "v100", "v101", "v102", "v103", "v104", "v105", "v106", "v107", "v108", "v109", "v110", "v111", "v112", "v113", "v114", "v115", "v116", "v117", "v118", "v119", "v120", "v121", "v122", "v123", "v124", "v125", "v126", "v127", "v128", "v129", "v130", "v131", "v132", "v133", "v134", "v135", "v136", "v137", "v138", "v139", "v140", "v141", "v142", "v143", "v144", "v145", "v146", "v147", "v148", "v149", "v150", "v151", "v152", "v153", "v154", "v155", "v156", "v157", "v158", "v159", "v160", "v161", "v162", "v163", "v164", "v165", "v166", "v167", "v168", "v169", "v170", "v171", "v172", "v173", "v174", "v175", "v176", "v177", "v178", "v179", "v180", "v181", "v182", "v183", "v184", "v185", "v186", "v187", "v188", "v189", "v190", "v191", "v192", "v193", "v194", "v195", "v196", "v197", "v198", "v199", "v200", "v201", "v202", "v203", "v204", "v205", "v206", "v207", "v208", "v209", "v210", "v211", "v212", "v213", "v214", "v215", "v216", "v217", "v218", "v219", "v220", "v221", "v222", "v223", "v224", "v225", "v226", "v227", "v228", "v229", "v230", "v231", "v232", "v233", "v234", "v235", "v236", "v237", "v238", "v239", "v240", "v241", "v242", "v243", "v244", "v245", "v246", "v247", "v248", "v249", "v250", "v251", "v252", "v253", "v254", "v255", "s4", "s5", "s6", "s7", "s8", "s9", "s10", "s11", "s12", "s13", "s14", "s15", "s16", "s17", "s18", "s19", "s20", "s21", "s22", "s23", "s24", "s25", "s26", "s27", "s28", "s29", "s30", "s31", "s32", "s33", "s34", "s35", "s36", "s37", "s38", "s39", "s40", "s41", "s42", "s43", "s44", "s45", "s46", "s47", "s48", "s49", "s50", "s51", "s52", "s53", "s54", "s55", "s56", "s57", "s58", "s59", "s60", "s61", "s62", "s63", "s64", "s65", "s66", "s67", "s68", "s69", "s70", "s71", "s72", "s73", "vcc", "memory");
}

extern "C" void kernel_launch(void* const* d_in, const int* in_sizes, int n_in, void* d_out, int out_size, void* d_ws, size_t ws_size, hipStream_t stream) {
    (void)hipFuncSetAttribute((const void*)fused_kernel, hipFuncAttributeMaxDynamicSharedMemorySize, LDS_BYTES);
    const unsigned F = 0x20000;
    fused_kernel<<<256, 512, LDS_BYTES, stream>>>(d_in[0], 2048 * 896, F, d_in[4], 448 * 3584, F, d_in[5], 448 * 3584, F, d_in[1], 448 * 2688 + 1024, F,
                                                  d_ws, WS_BYTES, F, d_in[6], 1792 * 896, F, d_in[2], d_in[4], d_out, d_in[5]);
}
```
